```python
import torch

def _softmax(x, axis=-1):
    return torch.softmax(x, dim=axis)
def _gelu(x):
    return torch.nn.functional.gelu(x, approximate="tanh")
def _silu(x):
    return torch.nn.functional.silu(x)
def _sigmoid(x):
    return torch.sigmoid(x)
def _layernorm(x, axis=-1, eps=1e-5):
    m = x.mean(dim=axis, keepdim=True)
    v = x.var(dim=axis, keepdim=True, unbiased=False)
    return (x - m) / torch.sqrt(v + eps)
def _scatter_add(tgt, idx, src, axis=0):
    return tgt.clone().index_add_(axis, idx, src)
def _segment_sum(src, seg, n):
    out = torch.zeros((n,) + src.shape[1:], dtype=src.dtype)
    return out.index_add_(0, seg, src)
def _pad(x, pw):
    flat = tuple(v for pair in reversed(pw) for v in pair)
    return torch.nn.functional.pad(x, flat)

kernel_name = 'concat_heads__gqa_block_cached__gqa_block_cached__near_ridge'

def setup_inputs():

    _w = (lambda a: (lambda t: t.to(torch.bfloat16) if t.is_floating_point() else t)(torch.as_tensor(a)))
    def _A_setup():
        B, Sq, D = 8, 80, 448
        Hq, Hkv, Sk, Dh = 16, 8, 1024, 256
        assert Hq % Hkv == 0, (Hq, Hkv)
        return {
            "x": _w((lambda *s: torch.randn(*s, dtype=torch.bfloat16) * 0.8)(B, Sq, D)),
            "k_cache": _w((lambda *s: torch.randn(*s, dtype=torch.bfloat16) * 0.8)(B, Hkv, Sk, Dh)),
            "v_cache": _w((lambda *s: torch.randn(*s, dtype=torch.bfloat16) * 0.05)(B, Hkv, Sk, Dh)),
            "wq": _w((lambda *s: torch.randn(*s, dtype=torch.bfloat16) * 0.8)(D, Hq * Dh) * (D ** -0.5)),
            "wo": _w((lambda *s: torch.randn(*s, dtype=torch.bfloat16) * 0.05)(Hq * Dh, D) * ((Hq * Dh) ** -0.5)),
        }
    _A_in = _A_setup()

    def _B_setup():
        B, Sq, D = 8, 80, 448
        Hq, Hkv, Sk, Dh = 16, 8, 1024, 256
        assert Hq % Hkv == 0, (Hq, Hkv)
        return {
            "x": _w((lambda *s: torch.randn(*s, dtype=torch.bfloat16) * 0.8)(B, Sq, D)),
            "k_cache": _w((lambda *s: torch.randn(*s, dtype=torch.bfloat16) * 0.8)(B, Hkv, Sk, Dh)),
            "v_cache": _w((lambda *s: torch.randn(*s, dtype=torch.bfloat16) * 0.05)(B, Hkv, Sk, Dh)),
            "wq": _w((lambda *s: torch.randn(*s, dtype=torch.bfloat16) * 0.8)(D, Hq * Dh) * (D ** -0.5)),
            "wo": _w((lambda *s: torch.randn(*s, dtype=torch.bfloat16) * 0.05)(Hq * Dh, D) * ((Hq * Dh) ** -0.5)),
        }
    _B_in = _B_setup()

    return {
        "A_x": _A_in["x"],
        "A_k_cache": _A_in["k_cache"],
        "A_v_cache": _A_in["v_cache"],
        "A_wq": _A_in["wq"],
        "A_wo": _A_in["wo"],
        "B_x": _B_in["x"],
        "B_k_cache": _B_in["k_cache"],
        "B_v_cache": _B_in["v_cache"],
        "B_wq": _B_in["wq"],
        "B_wo": _B_in["wo"],
    }

def reference(A_x, A_k_cache, A_v_cache, A_wq, A_wo, B_x, B_k_cache, B_v_cache, B_wq, B_wo):
    def _A_ref(x, k_cache, v_cache, wq, wo):
        B, Sq, D = x.shape
        Hq, Dh = 16, 256
        Hkv = k_cache.shape[1]
        rep = Hq // Hkv
        q = (lambda x, *d: x.permute(*d))((x @ wq).reshape(B, Sq, Hq, Dh), 0, 2, 1, 3)
        k = (lambda x, n, axis: torch.repeat_interleave(x, n, dim=axis))(k_cache, rep, axis=1)
        v = (lambda x, n, axis: torch.repeat_interleave(x, n, dim=axis))(v_cache, rep, axis=1)
        scores = torch.einsum("bhqd,bhkd->bhqk", q, k) * (Dh ** -0.5)
        p = _softmax(scores, axis=-1)
        o = torch.einsum("bhqk,bhkd->bhqd", p, v)
        return (lambda x, *d: x.permute(*d))(o, 0, 2, 1, 3).reshape(B, Sq, Hq * Dh) @ wo

    def _B_ref(x, k_cache, v_cache, wq, wo):
        B, Sq, D = x.shape
        Hq, Dh = 16, 256
        Hkv = k_cache.shape[1]
        rep = Hq // Hkv
        q = (lambda x, *d: x.permute(*d))((x @ wq).reshape(B, Sq, Hq, Dh), 0, 2, 1, 3)
        k = (lambda x, n, axis: torch.repeat_interleave(x, n, dim=axis))(k_cache, rep, axis=1)
        v = (lambda x, n, axis: torch.repeat_interleave(x, n, dim=axis))(v_cache, rep, axis=1)
        scores = torch.einsum("bhqd,bhkd->bhqk", q, k) * (Dh ** -0.5)
        p = _softmax(scores, axis=-1)
        o = torch.einsum("bhqk,bhkd->bhqd", p, v)
        return (lambda x, *d: x.permute(*d))(o, 0, 2, 1, 3).reshape(B, Sq, Hq * Dh) @ wo

    a = _A_ref(A_x, A_k_cache, A_v_cache, A_wq, A_wo)
    b = _B_ref(B_x, B_k_cache, B_v_cache, B_wq, B_wo)
    return torch.concatenate([a, b], axis=-1)
```

```cpp
#include <hip/hip_runtime.h>
#include <hip/hip_bf16.h>

typedef float f32x4 __attribute__((ext_vector_type(4)));
typedef float f32x2 __attribute__((ext_vector_type(2)));
typedef __bf16 bf16x8 __attribute__((ext_vector_type(8)));
typedef __bf16 bf16x4 __attribute__((ext_vector_type(4)));
typedef __bf16 bf16x2 __attribute__((ext_vector_type(2)));
typedef unsigned u32x4 __attribute__((ext_vector_type(4)));
typedef unsigned u32x2 __attribute__((ext_vector_type(2)));
typedef __amdgpu_buffer_rsrc_t srd_t;
#define DI __device__ __forceinline__
#define LDS3 __attribute__((address_space(3)))
#define SBAR() __builtin_amdgcn_sched_barrier(0)
#define MFMA16(a, b, c) __builtin_amdgcn_mfma_f32_16x16x32_bf16((a), (b), (c), 0, 0, 0)

#define KV_POL " nt"
#define W_POL ""
#define RESCALE_THR 8.0f

constexpr int NB = 8, SQ = 80, DM = 448, HQ = 16, HKV = 8, SK = 1024, DH = 256;
constexpr float SM_C = 0.0625f * 1.4426950408889634f;

constexpr unsigned KSLOT = 8448;
constexpr unsigned L_K = 0;
constexpr unsigned L_V = 4 * 2 * KSLOT;
constexpr unsigned L_P = L_V + 65536;
constexpr unsigned L_ST = L_P + 20480;
constexpr unsigned L_X = L_V;
constexpr unsigned L_QX = L_V;
constexpr unsigned LDS1 = 163840;

constexpr unsigned wq_slot(int s) { return (((s + 2) >> 1) & 1) * KSLOT + (s & 1) * 4096u; }

DI unsigned cvtpk(float lo, float hi) { f32x2 v = {lo, hi}; bf16x2 b = __builtin_convertvector(v, bf16x2); return __builtin_bit_cast(unsigned, b); }
DI bf16x8 pack8(const f32x4& a, const f32x4& b) { u32x4 w = {cvtpk(a[0], a[1]), cvtpk(a[2], a[3]), cvtpk(b[0], b[1]), cvtpk(b[2], b[3])}; return __builtin_bit_cast(bf16x8, w); }
DI bf16x8 lds_rd16(unsigned a) { return *(const LDS3 bf16x8*)(size_t)a; }
DI f32x4 lds_rdf4(unsigned a) { return *(const LDS3 f32x4*)(size_t)a; }
DI void lds_wr16(unsigned a, bf16x8 v) { *(LDS3 bf16x8*)(size_t)a = v; }
DI void lds_wrf4(unsigned a, f32x4 v) { *(LDS3 f32x4*)(size_t)a = v; }
DI void lds_wrf(unsigned a, float v) { *(LDS3 float*)(size_t)a = v; }
DI bf16x8 lds_tr2(unsigned a0, unsigned a1) {
  bf16x4 lo = __builtin_amdgcn_ds_read_tr16_b64_v4bf16((LDS3 bf16x4*)(size_t)a0);
  bf16x4 hi = __builtin_amdgcn_ds_read_tr16_b64_v4bf16((LDS3 bf16x4*)(size_t)a1);
  return __builtin_shufflevector(lo, hi, 0, 1, 2, 3, 4, 5, 6, 7);
}
DI srd_t mk_srd(const void* p, unsigned bytes) { return __builtin_amdgcn_make_buffer_rsrc((void*)p, 0, bytes, 0x00020000); }
template <int N> DI void wait_vm() { asm volatile("s_waitcnt vmcnt(%0)" ::"i"(N) : "memory"); }
DI void barrier_lds() { asm volatile("s_waitcnt lgkmcnt(0)\n\ts_barrier" ::: "memory"); }
DI float xmax_g(float x) {
  auto a = __builtin_amdgcn_permlane16_swap(__float_as_uint(x), __float_as_uint(x), false, false);
  x = fmaxf(__uint_as_float(a[0]), __uint_as_float(a[1]));
  auto b = __builtin_amdgcn_permlane32_swap(__float_as_uint(x), __float_as_uint(x), false, false);
  return fmaxf(__uint_as_float(b[0]), __uint_as_float(b[1]));
}
DI float xsum_g(float x) {
  auto a = __builtin_amdgcn_permlane16_swap(__float_as_uint(x), __float_as_uint(x), false, false);
  x = __uint_as_float(a[0]) + __uint_as_float(a[1]);
  auto b = __builtin_amdgcn_permlane32_swap(__float_as_uint(x), __float_as_uint(x), false, false);
  return __uint_as_float(b[0]) + __uint_as_float(b[1]);
}

DI void dma1(unsigned lds, unsigned voff, srd_t srd, unsigned soff) {
  asm volatile("s_mov_b32 m0, %0\n\ts_nop 0\n\tbuffer_load_dwordx4 %1, %2, %3 offen" W_POL " lds" ::"s"(lds), "v"(voff), "s"(srd), "s"(soff) : "memory");
}
DI void dma_k(unsigned lds, unsigned voff, srd_t srd, unsigned soff) {
  asm volatile(
      "s_mov_b32 m0, %0\n\ts_nop 0\n\tbuffer_load_dwordx4 %1, %2, %3 offen" KV_POL " lds\n\t"
      "s_add_u32 m0, m0, 32\n\ts_nop 0\n\tbuffer_load_dwordx4 %1, %2, %3 offen offset:1024" KV_POL " lds\n\t"
      "s_add_u32 m0, m0, 32\n\ts_nop 0\n\tbuffer_load_dwordx4 %1, %2, %3 offen offset:2048" KV_POL " lds\n\t"
      "s_add_u32 m0, m0, 32\n\ts_nop 0\n\tbuffer_load_dwordx4 %1, %2, %3 offen offset:3072" KV_POL " lds\n\t"
      "s_add_u32 m0, m0, 4128\n\ts_nop 0\n\tbuffer_load_dwordx4 %1, %2, %4 offen" KV_POL " lds\n\t"
      "s_add_u32 m0, m0, 32\n\ts_nop 0\n\tbuffer_load_dwordx4 %1, %2, %4 offen offset:1024" KV_POL " lds\n\t"
      "s_add_u32 m0, m0, 32\n\ts_nop 0\n\tbuffer_load_dwordx4 %1, %2, %4 offen offset:2048" KV_POL " lds\n\t"
      "s_add_u32 m0, m0, 32\n\ts_nop 0\n\tbuffer_load_dwordx4 %1, %2, %4 offen offset:3072" KV_POL " lds"
      ::"s"(lds), "v"(voff), "s"(srd), "s"(soff), "s"(soff + 4096u) : "memory", "scc");
}
#define DMA4(NAME, POL)                                                                                                   \
  template <unsigned STEP> DI void NAME(unsigned lds, unsigned voff, srd_t srd, unsigned soff) {                          \
    asm volatile("s_mov_b32 m0, %0\n\ts_nop 0\n\tbuffer_load_dwordx4 %1, %2, %3 offen" POL " lds\n\t"                     \
                 "buffer_load_dwordx4 %1, %2, %4 offen offset:1024" POL " lds\n\t"                                        \
                 "buffer_load_dwordx4 %1, %2, %5 offen offset:2048" POL " lds\n\t"                                        \
                 "buffer_load_dwordx4 %1, %2, %6 offen offset:3072" POL " lds"                                            \
                 ::"s"(lds), "v"(voff), "s"(srd), "s"(soff), "s"(soff + (STEP - 1024u)), "s"(soff + 2u * (STEP - 1024u)), \
                 "s"(soff + 3u * (STEP - 1024u)) : "memory");                                                             \
  }
DMA4(dma4_v, KV_POL)
DMA4(dma4_w, W_POL)

__global__ __launch_bounds__(256, 1) void attn_k1(const char* __restrict__ xA, const char* __restrict__ kA, const char* __restrict__ vA,
                                                  const char* __restrict__ wqA, const char* __restrict__ xB, const char* __restrict__ kB,
                                                  const char* __restrict__ vB, const char* __restrict__ wqB, char* __restrict__ ows) {
  __shared__ __attribute__((aligned(16))) char lds_mem[LDS1];
  const unsigned lb = (unsigned)(size_t)(LDS3 char*)lds_mem;
  const unsigned tid = threadIdx.x, lane = tid & 63u;
  const unsigned w = __builtin_amdgcn_readfirstlane(tid >> 6);
  const unsigned bid = blockIdx.x;
  const unsigned idx = bid >> 3, combo = 2u * (bid & 7u) + (idx >> 4);
  const unsigned br = combo >> 3, kvh = combo & 7u, b = (idx & 15u) >> 1, h = 2u * kvh + (idx & 1u);
  const unsigned r16 = lane & 15u, g = lane >> 4, q4 = r16 >> 2, p4 = lane & 3u;

  const srd_t sX = mk_srd((br ? xB : xA) + (size_t)b * (SQ * DM * 2), SQ * DM * 2);
  const srd_t sW = mk_srd((br ? wqB : wqA) + h * (DH * 2), DM * HQ * DH * 2 - h * (DH * 2));
  const srd_t sK = mk_srd((br ? kB : kA) + (size_t)(b * HKV + kvh) * (SK * DH * 2), SK * DH * 2);
  const srd_t sV = mk_srd((br ? vB : vA) + (size_t)(b * HKV + kvh) * (SK * DH * 2), SK * DH * 2);

  const unsigned kbase = lb + L_K + w * 2u * KSLOT;
  const unsigned vbase = lb + L_V + w * 16384u;
  {
#pragma unroll
    for (int i = 0; i < 18; ++i) {
      const unsigned n = 4u * i + w;
      if (n < 70u) {
        const unsigned c = n * 64u + lane, row = c / 56u, ch = c - row * 56u;
        dma1(lb + L_X + n * 1024u, row * 896u + ((ch ^ ((row >> 1) & 7u)) << 4), sX, 0u);
      }
    }
  }
  const unsigned wq_voff = (lane >> 3) * 8192u + (((lane & 7u) ^ (((lane >> 4) & 1u) << 2)) << 4) + w * 128u;
  const unsigned k_voff = (lane >> 5) * 512u + (((lane & 31u) ^ (lane >> 5)) << 4);
  const unsigned v_voff = (lane >> 3) * 512u + (((lane & 7u) ^ (((lane >> 4) & 3u) << 1)) << 4) + w * 128u;
#pragma unroll
  for (int s = 0; s < 4; ++s) dma4_w<65536>(kbase + wq_slot(s), wq_voff, sW, s * 262144u);

  f32x4 qa[2][2][5];
#pragma unroll
  for (int u = 0; u < 2; ++u)
#pragma unroll
    for (int t = 0; t < 2; ++t)
#pragma unroll
      for (int m = 0; m < 5; ++m) qa[u][t][m] = f32x4{0.f, 0.f, 0.f, 0.f};
  {
    const unsigned xr0 = lb + L_X + r16 * 896u + ((g ^ ((r16 >> 1) & 7u)) << 4);
    const unsigned xr1 = lb + L_X + r16 * 896u + (((4u + g) ^ ((r16 >> 1) & 7u)) << 4);
    const unsigned wr0 = kbase + (8u * g + q4) * 128u + ((4u * (0u ^ (q4 >> 1)) + p4) << 4);
    const unsigned wr1 = kbase + (8u * g + q4) * 128u + ((4u * (1u ^ (q4 >> 1)) + p4) << 4);
    wait_vm<16>();
    barrier_lds();
#pragma unroll
    for (int s = 0; s < 14; ++s) {
      if (s <= 10) wait_vm<12>(); else if (s == 11) wait_vm<8>(); else if (s == 12) wait_vm<12>(); else wait_vm<8>();
      const unsigned so = wq_slot(s);
      bf16x8 xf[5], wf[2][2];
#pragma unroll
      for (int u = 0; u < 2; ++u)
#pragma unroll
        for (int t = 0; t < 2; ++t) { const unsigned a = (u ? wr1 : wr0) + so + t * 8u; wf[u][t] = lds_tr2(a, a + 512u); }
#pragma unroll
      for (int m = 0; m < 5; ++m) xf[m] = lds_rd16(((s & 1) ? xr1 : xr0) + (s >> 1) * 128u + m * 14336u);
#pragma unroll
      for (int u = 0; u < 2; ++u)
#pragma unroll
        for (int t = 0; t < 2; ++t)
#pragma unroll
          for (int m = 0; m < 5; ++m) qa[u][t][m] = MFMA16(wf[u][t], xf[m], qa[u][t][m]);
      SBAR();
      if (s + 4 < 14) dma4_w<65536>(kbase + so, wq_voff, sW, (s + 4) * 262144u);
      if (s == 11) dma_k(kbase, k_voff, sK, (32u * w) * 512u);
      if (s == 13) dma_k(kbase + KSLOT, k_voff, sK, (32u * w + 16u) * 512u);
      SBAR();
    }
  }
  barrier_lds();
#pragma unroll
  for (int u = 0; u < 2; ++u)
#pragma unroll
    for (int m = 0; m < 5; ++m) lds_wr16(lb + L_QX + ((2u * w + u) * 5u + m) * 1024u + lane * 16u, pack8(qa[u][0][m], qa[u][1][m]));
  barrier_lds();
  bf16x8 Q[8][5];
#pragma unroll
  for (int s = 0; s < 8; ++s)
#pragma unroll
    for (int m = 0; m < 5; ++m) Q[s][m] = lds_rd16(lb + L_QX + (s * 5 + m) * 1024u + lane * 16u);
  barrier_lds();
#pragma unroll
  for (int c = 0; c < 4; ++c) dma4_v<4096>(vbase + c * 4096u, v_voff, sV, (32u * c) * 512u);

  const unsigned kr = kbase + (r16 >> 1) * 1056u + (r16 & 1u) * 512u + ((g ^ (r16 & 1u)) << 4);
  const unsigned fv = (2u * g + (q4 >> 1)) & 3u;
  unsigned vr[4];
#pragma unroll
  for (int c = 0; c < 4; ++c) vr[c] = vbase + (4u * g + q4) * 128u + ((((c ^ fv) << 1) | (p4 >> 1)) << 4) + (p4 & 1u) * 8u;
  const unsigned pa = lb + L_P + lane * 16u;
  const unsigned sa = lb + L_ST + r16 * 16u;

  f32x4 O[4][5];
#pragma unroll
  for (int c = 0; c < 4; ++c)
#pragma unroll
    for (int m = 0; m < 5; ++m) O[c][m] = f32x4{0.f, 0.f, 0.f, 0.f};
  float mrow[5], lrow[5];
  bf16x8 P[5];
#pragma unroll
  for (int m = 0; m < 5; ++m) { mrow[m] = -__builtin_inff(); lrow[m] = 0.f; P[m] = bf16x8{}; }

#pragma unroll 1
  for (int j = 0; j <= 8; ++j) {
    f32x4 S[2][5];
    if (j > 0) {
#pragma unroll
      for (int m = 0; m < 5; ++m) lds_wr16(pa + (w * 5u + m) * 1024u, P[m]);
    }
    if (j < 8) {
#pragma unroll
      for (int u = 0; u < 2; ++u) {
        if (j == 1) wait_vm<8>(); else wait_vm<24>();
        bf16x8 kf[8];
#pragma unroll
        for (int s = 0; s < 8; ++s) kf[s] = lds_rd16(kr + u * KSLOT + s * 64u);
#pragma unroll
        for (int m = 0; m < 5; ++m) S[u][m] = f32x4{0.f, 0.f, 0.f, 0.f};
#pragma unroll
        for (int s = 0; s < 8; ++s)
#pragma unroll
          for (int m = 0; m < 5; ++m) S[u][m] = MFMA16(kf[s], Q[s][m], S[u][m]);
        SBAR();
        dma_k(kbase + u * KSLOT, k_voff, sK, (128u * (j + 1) + 32u * w + 16u * u) * 512u);
        SBAR();
      }
#pragma unroll
      for (int m = 0; m < 5; ++m) {
        float mx = fmaxf(fmaxf(fmaxf(S[0][m][0], S[0][m][1]), fmaxf(S[0][m][2], S[0][m][3])), fmaxf(fmaxf(S[1][m][0], S[1][m][1]), fmaxf(S[1][m][2], S[1][m][3])));
        mx = xmax_g(mx);
        lds_wrf(sa + m * 256u + w * 4u, mx);
      }
    } else {
#pragma unroll
      for (int m = 0; m < 5; ++m) lds_wrf(sa + m * 256u + w * 4u, xsum_g(lrow[m]));
    }
    barrier_lds();
    float al[5];
    bool resc = false;
    if (j < 8) {
      float cm[5];
      bool grow = false;
#pragma unroll
      for (int m = 0; m < 5; ++m) {
        const f32x4 st = lds_rdf4(sa + m * 256u);
        cm[m] = fmaxf(fmaxf(st[0], st[1]), fmaxf(st[2], st[3]));
        grow |= (cm[m] - mrow[m]) * SM_C > RESCALE_THR;
        al[m] = 1.f;
      }
      resc = __any(grow);
      if (resc) {
#pragma unroll
        for (int m = 0; m < 5; ++m) {
          const float mn = fmaxf(mrow[m], cm[m]);
          al[m] = __builtin_amdgcn_exp2f((mrow[m] - mn) * SM_C);
          mrow[m] = mn;
        }
      }
    }
    if (j > 0) {
#pragma unroll
      for (int c = 0; c < 4; ++c) {
        if (j == 8) wait_vm<12>(); else wait_vm<28>();
        bf16x8 vf[4], pf[5];
#pragma unroll
        for (int d = 0; d < 4; ++d) vf[d] = lds_tr2(vr[d] + c * 4096u, vr[d] + c * 4096u + 2048u);
#pragma unroll
        for (int m = 0; m < 5; ++m) pf[m] = lds_rd16(pa + (c * 5 + m) * 1024u);
#pragma unroll
        for (int d = 0; d < 4; ++d)
#pragma unroll
          for (int m = 0; m < 5; ++m) O[d][m] = MFMA16(vf[d], pf[m], O[d][m]);
        SBAR();
        dma4_v<4096>(vbase + c * 4096u, v_voff, sV, (128u * j + 32u * c) * 512u);
        SBAR();
      }
    }
    if (j < 8) {
#pragma unroll
      for (int m = 0; m < 5; ++m) {
        const float nm = -mrow[m] * SM_C;
        f32x4 e0, e1;
#pragma unroll
        for (int r = 0; r < 4; ++r) {
          e0[r] = __builtin_amdgcn_exp2f(fmaf(S[0][m][r], SM_C, nm));
          e1[r] = __builtin_amdgcn_exp2f(fmaf(S[1][m][r], SM_C, nm));
        }
        const float rs = ((e0[0] + e0[1]) + (e0[2] + e0[3])) + ((e1[0] + e1[1]) + (e1[2] + e1[3]));
        lrow[m] = fmaf(lrow[m], al[m], rs);
        P[m] = pack8(e0, e1);
      }
      if (resc && j > 0) {
#pragma unroll
        for (int d = 0; d < 4; ++d)
#pragma unroll
          for (int m = 0; m < 5; ++m) O[d][m] *= al[m];
      }
      barrier_lds();
    }
  }
  char* op = ows + (size_t)((br * NB + b) * HQ + h) * 40960u + lane * 16u;
#pragma unroll
  for (int m = 0; m < 5; ++m) {
    const f32x4 st = lds_rdf4(sa + m * 256u);
    const float inv = 1.0f / ((st[0] + st[1]) + (st[2] + st[3]));
#pragma unroll
    for (int u = 0; u < 2; ++u) *(bf16x8*)(op + ((2u * w + u) * 5u + m) * 1024u) = pack8(O[2 * u][m] * inv, O[2 * u + 1][m] * inv);
  }
}

__global__ __launch_bounds__(256, 1) void oproj_k2(const char* __restrict__ woA, const char* __restrict__ woB, const char* __restrict__ ows,
                                                   float* __restrict__ part) {
  __shared__ __attribute__((aligned(16))) char lds_mem[143360];
  const unsigned lb = (unsigned)(size_t)(LDS3 char*)lds_mem;
  const unsigned tid = threadIdx.x, lane = tid & 63u;
  const unsigned w = __builtin_amdgcn_readfirstlane(tid >> 6);
  const unsigned bid = blockIdx.x, xg = bid & 7u, j = bid >> 3;
  const unsigned br = xg >> 2, kq = xg & 3u, b = j >> 2, nq = j & 3u, h = 4u * kq + w;
  const unsigned r16 = lane & 15u, g = lane >> 4, q4 = r16 >> 2, p4 = lane & 3u;
  const srd_t sW = mk_srd((br ? woB : woA) + nq * 224u, HQ * DH * DM * 2 - nq * 224u);
  const unsigned ring = lb + w * 35840u;
  bf16x8 of[8][5];
  const char* op = ows + (size_t)((br * NB + b) * HQ + h) * 40960u + lane * 16u;
#pragma unroll
  for (int s = 0; s < 8; ++s)
#pragma unroll
    for (int m = 0; m < 5; ++m) of[s][m] = *(const bf16x8*)(op + (s * 5 + m) * 1024u);
  unsigned voff[7];
#pragma unroll
  for (int i = 0; i < 7; ++i) { const unsigned n = i * 64u + lane, row = n / 14u, ch = n - row * 14u; voff[i] = row * 896u + ch * 16u; }
  auto issue = [&](int s) {
#pragma unroll
    for (int i = 0; i < 7; ++i) dma1(ring + (s % 5) * 7168u + i * 1024u, voff[i], sW, (h * 256u + 32u * s) * 896u);
  };
#pragma unroll
  for (int s = 0; s < 5; ++s) issue(s);
  f32x4 acc[7][5];
#pragma unroll
  for (int n = 0; n < 7; ++n)
#pragma unroll
    for (int m = 0; m < 5; ++m) acc[n][m] = f32x4{0.f, 0.f, 0.f, 0.f};
  const unsigned ar = ring + (4u * g + q4) * 224u + p4 * 8u;
#pragma unroll
  for (int s = 0; s < 8; ++s) {
    if (s <= 3) wait_vm<28>(); else if (s == 4) wait_vm<21>(); else if (s == 5) wait_vm<14>(); else if (s == 6) wait_vm<7>(); else wait_vm<0>();
    bf16x8 wf[7];
#pragma unroll
    for (int n = 0; n < 7; ++n) { const unsigned a = ar + (s % 5) * 7168u + n * 32u; wf[n] = lds_tr2(a, a + 3584u); }
#pragma unroll
    for (int n = 0; n < 7; ++n)
#pragma unroll
      for (int m = 0; m < 5; ++m) acc[n][m] = MFMA16(wf[n], of[s][m], acc[n][m]);
    SBAR();
    if (s + 5 < 8) issue(s + 5);
    SBAR();
  }
#pragma unroll
  for (int n = 0; n < 7; ++n)
#pragma unroll
    for (int m = 0; m < 5; ++m) lds_wrf4(ring + (n * 5 + m) * 1024u + lane * 16u, acc[n][m]);
  barrier_lds();
  f32x4* dst = (f32x4*)part + (size_t)((((kq * 2u + br) * NB + b) * 4u + nq) * 2240u);
#pragma unroll
  for (int i = 0; i < 9; ++i) {
    const unsigned e = i * 256u + tid;
    if (e < 2240u) {
      const unsigned a = lb + e * 16u;
      dst[e] = (lds_rdf4(a) + lds_rdf4(a + 35840u)) + (lds_rdf4(a + 71680u) + lds_rdf4(a + 107520u));
    }
  }
}

__global__ __launch_bounds__(256) void reduce_k3(const float* __restrict__ part, char* __restrict__ out) {
  const unsigned e = blockIdx.x * 256u + threadIdx.x;
  const f32x4* p = (const f32x4*)part + e;
  const f32x4 v = (p[0] + p[143360]) + (p[2 * 143360] + p[3 * 143360]);
  const unsigned grp = e / 2240u, i = e - grp * 2240u, tile = i >> 6, lane = i & 63u;
  const unsigned nq = grp & 3u, b = (grp >> 2) & 7u, br = grp >> 5, n = tile / 5u, m = tile - n * 5u;
  const unsigned row = b * SQ + 16u * m + (lane & 15u), col = br * DM + nq * 112u + 16u * n + 4u * (lane >> 4);
  *(u32x2*)(out + ((size_t)row * (2 * DM) + col) * 2) = u32x2{cvtpk(v[0], v[1]), cvtpk(v[2], v[3])};
}

extern "C" void kernel_launch(void* const* d_in, const int* in_sizes, int n_in, void* d_out, int out_size, void* d_ws, size_t ws_size,
                              hipStream_t stream) {
  char* ows = (char*)d_ws;
  float* part = (float*)((char*)d_ws + 256u * 40960u);
  attn_k1<<<256, 256, 0, stream>>>((const char*)d_in[0], (const char*)d_in[1], (const char*)d_in[2], (const char*)d_in[3],
                                   (const char*)d_in[5], (const char*)d_in[6], (const char*)d_in[7], (const char*)d_in[8], ows);
  oproj_k2<<<256, 256, 0, stream>>>((const char*)d_in[4], (const char*)d_in[9], ows, part);
  reduce_k3<<<560, 256, 0, stream>>>(part, (char*)d_out);
}
```

```cpp
#include <hip/hip_runtime.h>
#include <hip/hip_bf16.h>
#include <utility>

typedef float f32x4 __attribute__((ext_vector_type(4)));
typedef float f32x2 __attribute__((ext_vector_type(2)));
typedef __bf16 bf16x8 __attribute__((ext_vector_type(8)));
typedef __bf16 bf16x4 __attribute__((ext_vector_type(4)));
typedef __bf16 bf16x2 __attribute__((ext_vector_type(2)));
typedef unsigned u32x4 __attribute__((ext_vector_type(4)));
typedef unsigned u32x2 __attribute__((ext_vector_type(2)));
typedef __amdgpu_buffer_rsrc_t srd_t;
#define DI __device__ __forceinline__
#define LDS3 __attribute__((address_space(3)))
#define SBAR() __builtin_amdgcn_sched_barrier(0)
#define MFMA16(a, b, c) __builtin_amdgcn_mfma_f32_16x16x32_bf16((a), (b), (c), 0, 0, 0)

#define KV_POL " nt"
#define W_POL " nt"
#define RESCALE_THR 8.0f

#define STAMP(i) do {} while (0)
#define STAMP_FLUSH() do {} while (0)

constexpr int NB = 8, SQ = 80, DM = 448, HQ = 16, HKV = 8, SK = 1024, DH = 256;
constexpr float SM_C = 0.0625f * 1.4426950408889634f;

constexpr unsigned KSLOT = 8448;
constexpr unsigned L_K = 0;
constexpr unsigned L_V = 4 * 2 * KSLOT;
constexpr unsigned L_P = L_V + 65536;
constexpr unsigned L_ST = L_P + 20480;
constexpr unsigned L_X = L_V;
constexpr unsigned L_MX = L_ST + 1280;
constexpr unsigned L_SCR = L_MX + 512;
constexpr unsigned L_QX = L_V;
constexpr unsigned LDS1 = 163840;

constexpr unsigned wq_slot(int s) { return ((s + 2) & 3) * 4224u; }

DI unsigned cvtpk(float lo, float hi) { f32x2 v = {lo, hi}; bf16x2 b = __builtin_convertvector(v, bf16x2); return __builtin_bit_cast(unsigned, b); }
typedef u32x4 frag;
DI frag pack8(const f32x4& a, const f32x4& b) { return u32x4{cvtpk(a[0], a[1]), cvtpk(a[2], a[3]), cvtpk(b[0], b[1]), cvtpk(b[2], b[3])}; }
DI frag lds_rd16(unsigned a) { return *(const LDS3 frag*)(size_t)a; }
DI f32x4 lds_rdf4(unsigned a) { return *(const LDS3 f32x4*)(size_t)a; }
DI void lds_wr16(unsigned a, frag v) { *(LDS3 frag*)(size_t)a = v; }
DI void lds_wrf4(unsigned a, f32x4 v) { *(LDS3 f32x4*)(size_t)a = v; }
DI void lds_wrf(unsigned a, float v) { *(LDS3 float*)(size_t)a = v; }
DI frag lds_tr2(unsigned a0, unsigned a1) {
  const u32x2 lo = __builtin_bit_cast(u32x2, __builtin_amdgcn_ds_read_tr16_b64_v4bf16((LDS3 bf16x4*)(size_t)a0));
  const u32x2 hi = __builtin_bit_cast(u32x2, __builtin_amdgcn_ds_read_tr16_b64_v4bf16((LDS3 bf16x4*)(size_t)a1));
  return __builtin_shufflevector(lo, hi, 0, 1, 2, 3);
}
DI u32x2 lds_tr1(unsigned a) { return __builtin_bit_cast(u32x2, __builtin_amdgcn_ds_read_tr16_b64_v4bf16((LDS3 bf16x4*)(size_t)a)); }
DI float lds_rdf(unsigned a) { return *(const LDS3 float*)(size_t)a; }
template <int OFF> DI void lds_max(unsigned a, float v) { asm volatile("ds_max_f32 %0, %1 offset:%2" ::"v"(a), "v"(v), "i"(OFF) : "memory"); }
DI srd_t mk_srd(const void* p, unsigned bytes) { return __builtin_amdgcn_make_buffer_rsrc((void*)p, 0, bytes, 0x00020000); }
template <int N> DI void wait_vm() { asm volatile("s_waitcnt vmcnt(%0)" ::"i"(N) : "memory"); }
DI void barrier_lds() { asm volatile("s_waitcnt lgkmcnt(0)\n\ts_barrier" ::: "memory"); }
DI float xmax_g(float x) {
  auto a = __builtin_amdgcn_permlane16_swap(__float_as_uint(x), __float_as_uint(x), false, false);
  x = fmaxf(__uint_as_float(a[0]), __uint_as_float(a[1]));
  auto b = __builtin_amdgcn_permlane32_swap(__float_as_uint(x), __float_as_uint(x), false, false);
  return fmaxf(__uint_as_float(b[0]), __uint_as_float(b[1]));
}
DI float xsum_g(float x) {
  auto a = __builtin_amdgcn_permlane16_swap(__float_as_uint(x), __float_as_uint(x), false, false);
  x = __uint_as_float(a[0]) + __uint_as_float(a[1]);
  auto b = __builtin_amdgcn_permlane32_swap(__float_as_uint(x), __float_as_uint(x), false, false);
  return __uint_as_float(b[0]) + __uint_as_float(b[1]);
}

DI void dma1(unsigned lds, unsigned voff, srd_t srd, unsigned soff) {
  asm volatile("s_mov_b32 m0, %0\n\ts_nop 0\n\tbuffer_load_dwordx4 %1, %2, %3 offen" W_POL " lds" ::"s"(lds), "v"(voff), "s"(srd), "s"(soff) : "memory");
}
DI void dma_k(unsigned lds, unsigned voff, srd_t srd, unsigned soff) {
  asm volatile(
      "s_mov_b32 m0, %0\n\ts_nop 0\n\tbuffer_load_dwordx4 %1, %2, %3 offen" KV_POL " lds\n\t"
      "s_add_u32 m0, m0, 32\n\ts_nop 0\n\tbuffer_load_dwordx4 %1, %2, %3 offen offset:1024" KV_POL " lds\n\t"
      "s_add_u32 m0, m0, 32\n\ts_nop 0\n\tbuffer_load_dwordx4 %1, %2, %3 offen offset:2048" KV_POL " lds\n\t"
      "s_add_u32 m0, m0, 32\n\ts_nop 0\n\tbuffer_load_dwordx4 %1, %2, %3 offen offset:3072" KV_POL " lds\n\t"
      "s_add_u32 m0, m0, 4128\n\ts_nop 0\n\tbuffer_load_dwordx4 %1, %2, %4 offen" KV_POL " lds\n\t"
      "s_add_u32 m0, m0, 32\n\ts_nop 0\n\tbuffer_load_dwordx4 %1, %2, %4 offen offset:1024" KV_POL " lds\n\t"
      "s_add_u32 m0, m0, 32\n\ts_nop 0\n\tbuffer_load_dwordx4 %1, %2, %4 offen offset:2048" KV_POL " lds\n\t"
      "s_add_u32 m0, m0, 32\n\ts_nop 0\n\tbuffer_load_dwordx4 %1, %2, %4 offen offset:3072" KV_POL " lds"
      ::"s"(lds), "v"(voff), "s"(srd), "s"(soff), "s"(soff + 4096u) : "memory", "scc");
}
#define DMA4(NAME, POL)                                                                                                   \
  template <unsigned STEP> DI void NAME(unsigned lds, unsigned voff, srd_t srd, unsigned soff) {                          \
    asm volatile("s_mov_b32 m0, %0\n\ts_nop 0\n\tbuffer_load_dwordx4 %1, %2, %3 offen" POL " lds\n\t"                     \
                 "buffer_load_dwordx4 %1, %2, %4 offen offset:1024" POL " lds\n\t"                                        \
                 "buffer_load_dwordx4 %1, %2, %5 offen offset:2048" POL " lds\n\t"                                        \
                 "buffer_load_dwordx4 %1, %2, %6 offen offset:3072" POL " lds"                                            \
                 ::"s"(lds), "v"(voff), "s"(srd), "s"(soff), "s"(soff + (STEP - 1024u)), "s"(soff + 2u * (STEP - 1024u)), \
                 "s"(soff + 3u * (STEP - 1024u)) : "memory");                                                             \
  }
DMA4(dma4_v, KV_POL)
DMA4(dma4_w, W_POL)

template <int IMM> DI void dma_kv(unsigned lds, unsigned voff, srd_t srd, unsigned soff) {
  asm volatile("s_mov_b32 m0, %0\n\ts_nop 0\n\tbuffer_load_dwordx4 %1, %2, %3 offen offset:%4" KV_POL " lds" ::"s"(lds), "v"(voff), "s"(srd), "s"(soff), "i"(IMM) : "memory");
}
template <int IMM> DI void dma_w(unsigned lds, unsigned voff, srd_t srd, unsigned soff) {
  asm volatile("s_mov_b32 m0, %0\n\ts_nop 0\n\tbuffer_load_dwordx4 %1, %2, %3 offen offset:%4" W_POL " lds" ::"s"(lds), "v"(voff), "s"(srd), "s"(soff), "i"(IMM) : "memory");
}
template <int K, int NA, int NB> DI void wait_vm_eq(unsigned v) {
  asm volatile("s_cmp_lg_u32 %0, %3\n\ts_cbranch_scc1 1f\n\ts_waitcnt vmcnt(%1)\n1:\n\ts_waitcnt vmcnt(%2)" ::"s"(v), "i"(NA), "i"(NB), "i"(K) : "memory", "scc");
}
template <int K, int NA> DI void wait_vm_ne(unsigned v) {
  asm volatile("s_cmp_eq_u32 %0, %2\n\ts_cbranch_scc1 1f\n\ts_waitcnt vmcnt(%1)\n1:" ::"s"(v), "i"(NA), "i"(K) : "memory", "scc");
}
template <int IMM> DI void dma_kv_if(unsigned c, unsigned lds, unsigned voff, srd_t srd, unsigned soff) {
  asm volatile("s_cmp_eq_u32 %5, 0\n\ts_cbranch_scc1 1f\n\ts_mov_b32 m0, %0\n\ts_nop 0\n\tbuffer_load_dwordx4 %1, %2, %3 offen offset:%4" KV_POL " lds\n1:"
               ::"s"(lds), "v"(voff), "s"(srd), "s"(soff), "i"(IMM), "s"(c) : "memory", "scc");
}
template <int I> struct IC { static constexpr int value = I; };
template <class F, int... Is> DI void for_seq(F&& f, std::integer_sequence<int, Is...>) { (f(IC<Is>{}), ...); }
template <class F> DI void for20(F&& f) { for_seq(f, std::make_integer_sequence<int, 20>{}); }
DI float max3f(float a, float b, float c) { float r; asm("v_max3_f32 %0, %1, %2, %3" : "=v"(r) : "v"(a), "v"(b), "v"(c)); return r; }
DI float max2f(float a, float b) { float r; asm("v_max_f32_e32 %0, %1, %2" : "=v"(r) : "v"(a), "v"(b)); return r; }
DI float addf(float a, float b) { float r; asm("v_add_f32_e32 %0, %1, %2" : "=v"(r) : "v"(a), "v"(b)); return r; }
struct TT { static constexpr bool value = true; };
struct FF { static constexpr bool value = false; };
DI void mm_va0(f32x4& d, const frag& a, const frag& bq) { asm volatile("v_mfma_f32_16x16x32_bf16 %0, %1, %2, 0" : "=&v"(d) : "v"(a), "a"(bq)); }
DI void mm_va(f32x4& d, const frag& a, const frag& bq) { asm volatile("v_mfma_f32_16x16x32_bf16 %0, %1, %2, %0" : "+v"(d) : "v"(a), "a"(bq)); }
DI void mm_av(f32x4& d, const frag& a, const frag& b) { asm volatile("v_mfma_f32_16x16x32_bf16 %0, %1, %2, %0" : "+a"(d) : "v"(a), "v"(b)); }
DI void mm_vv(f32x4& d, const frag& a, const frag& b) { asm volatile("v_mfma_f32_16x16x32_bf16 %0, %1, %2, %0" : "+v"(d) : "v"(a), "v"(b)); }
template <int OFF> DI void lds_rd16_acc(frag& d, unsigned a) { asm volatile("ds_read_b128 %0, %1 offset:%2" : "=a"(d) : "v"(a), "i"(OFF) : "memory"); }

__global__ __launch_bounds__(256, 1) void attn_k1(const char* __restrict__ xA, const char* __restrict__ kA, const char* __restrict__ vA,
                                                  const char* __restrict__ wqA, const char* __restrict__ xB, const char* __restrict__ kB,
                                                  const char* __restrict__ vB, const char* __restrict__ wqB, char* __restrict__ ows) {
  __shared__ __attribute__((aligned(16))) char lds_mem[LDS1];
  const unsigned lb = (unsigned)(size_t)(LDS3 char*)lds_mem;
  const unsigned tid = threadIdx.x, lane = tid & 63u;
  const unsigned w = __builtin_amdgcn_readfirstlane(tid >> 6);
  const unsigned bid = blockIdx.x;
  asm volatile("" ::"s"(xA), "s"(kA), "s"(vA), "s"(wqA), "s"(xB), "s"(kB), "s"(vB), "s"(wqB), "s"(ows));
#define MAP 0
  const unsigned idx = bid >> 3, xg = bid & 7u;
  const unsigned combo = 2u * xg + (idx >> 4), br = combo >> 3, kvh = combo & 7u, b = (idx & 15u) >> 1, rsel = b;
  const unsigned h = 2u * kvh + (idx & 1u);
  const unsigned r16 = lane & 15u, g = lane >> 4, q4 = r16 >> 2, p4 = lane & 3u;

  const srd_t sX = mk_srd((br ? xB : xA) + (size_t)b * (SQ * DM * 2), SQ * DM * 2);
  const srd_t sW = mk_srd((br ? wqB : wqA) + h * (DH * 2), DM * HQ * DH * 2 - h * (DH * 2));
  const srd_t sK = mk_srd((br ? kB : kA) + (size_t)(b * HKV + kvh) * (SK * DH * 2), SK * DH * 2);
  const srd_t sV = mk_srd((br ? vB : vA) + (size_t)(b * HKV + kvh) * (SK * DH * 2), SK * DH * 2);
  STAMP(0);
  const unsigned kbase = lb + L_K + w * 2u * KSLOT;
  const unsigned vbase = lb + L_V + w * 16384u;
  const unsigned rot = 0;
  const unsigned wq_voff = (lane >> 3) * 8192u + (((lane & 7u) ^ (((lane >> 4) & 1u) << 2)) << 4) + w * 128u;
  const unsigned k_voff = (lane >> 5) * 512u + (((lane & 31u) ^ (lane >> 5)) << 4);
  const unsigned v_voff = (lane >> 3) * 512u + (((lane & 7u) ^ (((lane >> 4) & 3u) << 1)) << 4) + w * 128u;
  const unsigned x_voff = (lane >> 3) * 896u + (((lane & 7u) ^ ((4u * (w & 1u) + (lane >> 4)) & 7u)) << 4);
  const unsigned x2l = w < 2u ? lb + L_X + (8u + w) * 1024u : lb + L_SCR, x2m = w < 2u ? 10240u : 0u, x2s = w < 2u ? (8u + w) * 7168u : 0x40000000u;
  auto xpiece = [&](unsigned pb, unsigned poison, auto t_c) {
    constexpr unsigned T = decltype(t_c)::value;
    if constexpr (T < 2) dma_w<0>(lb + L_X + pb * 10240u + (4u * T + w) * 1024u, x_voff, sX, (4u * T + w) * 7168u + pb * 128u + poison);
    else dma_w<0>(x2l + pb * x2m, x_voff, sX, x2s + pb * 128u + poison);
  };
  auto xblock = [&](unsigned pb, unsigned poison) { xpiece(pb, poison, IC<0>{}); xpiece(pb, poison, IC<1>{}); xpiece(pb, poison, IC<2>{}); };
  unsigned p0 = rot, p1 = rot == 6u ? 0u : rot + 1u;
  xblock(p0, 0u);
  dma4_w<65536>(kbase + wq_slot(0), wq_voff, sW, p0 * 524288u);
  dma4_w<65536>(kbase + wq_slot(1), wq_voff, sW, p0 * 524288u + 262144u);
  xblock(p1, 0u);
  dma4_w<65536>(kbase + wq_slot(2), wq_voff, sW, p1 * 524288u);
  dma4_w<65536>(kbase + wq_slot(3), wq_voff, sW, p1 * 524288u + 262144u);
  f32x4 qa[2][2][5];
#pragma unroll
  for (int u = 0; u < 2; ++u)
#pragma unroll
    for (int t = 0; t < 2; ++t)
#pragma unroll
      for (int m = 0; m < 5; ++m) { qa[u][t][m] = f32x4{0.f, 0.f, 0.f, 0.f}; asm volatile("" : "+v"(qa[u][t][m])); }
  f32x4 O[4][5];
#pragma unroll
  for (int c = 0; c < 4; ++c)
#pragma unroll
    for (int m = 0; m < 5; ++m) { O[c][m] = f32x4{0.f, 0.f, 0.f, 0.f}; asm volatile("" : "+a"(O[c][m])); }
  {
    unsigned xr0 = lb + L_X + r16 * 128u + ((g ^ (r16 >> 1)) << 4), xr1 = xr0 ^ 64u;
    unsigned wr0 = kbase + 8448u + (8u * g + q4) * 128u + ((4u * (0u ^ (q4 >> 1)) + p4) << 4);
    unsigned wr1 = kbase + 8448u + (8u * g + q4) * 128u + ((4u * (1u ^ (q4 >> 1)) + p4) << 4);
    frag xa[5], xb[5];
    u32x2 wa[8], wb[8];
    auto rd1 = [&](frag (&xf)[5], u32x2 (&wf)[8], unsigned xs, unsigned a0, unsigned a1, auto i_c) {
      constexpr int I = decltype(i_c)::value;
      if constexpr (I < 8) wf[I] = lds_tr1(((I >> 2) ? a1 : a0) + ((I >> 1) & 1) * 8u + (I & 1) * 512u);
      else if constexpr (I < 13) xf[I - 8] = lds_rd16(xs + (I - 8) * 2048u);
    };
    auto step = [&](frag (&xf)[5], u32x2 (&wf)[8], auto fill) {
      for20([&](auto i_c) {
        constexpr int I = decltype(i_c)::value, m = I / 4, ut = I % 4;
        mm_vv(qa[ut >> 1][ut & 1][m], __builtin_shufflevector(wf[2 * ut], wf[2 * ut + 1], 0, 1, 2, 3), xf[m]);
        fill(i_c); SBAR();
      });
    };
    if (tid < 80u) lds_wrf(lb + L_MX + tid * 4u, -__builtin_inff());
    STAMP(23);
    wait_vm<15>();
    barrier_lds();
    STAMP(1);
    for20([&](auto i_c) { rd1(xa, wa, xr0 + p0 * 10240u, wr0, wr1, i_c); });
    unsigned tog = 8448u;
#pragma unroll 1
    for (unsigned pp = 0; pp < 7u; ++pp) {
      if (pp == 1u) STAMP(28); if (pp == 3u) STAMP(29); if (pp == 5u) STAMP(30);
      const bool more = pp < 5u;
      const srd_t srd = more ? sW : sK;
      const unsigned voff = more ? wq_voff : k_voff, dm = more ? 0u : 32u, ds = more ? 64512u : 0u;
      const unsigned p2 = p1 == 6u ? 0u : p1 + 1u;
      const unsigned so = more ? p2 * 524288u : (32u * w) * 512u + (pp - 5u) * 8192u, so2 = so + (more ? 262144u : 4096u);
      const unsigned xb2 = more ? p2 : 7u, poison = more ? 0u : 0x40000000u;
      const unsigned xs1 = xr1 + p0 * 10240u, xs0 = xr0 + p1 * 10240u, slot = kbase + tog;
      wait_vm<11>();
      SBAR();
      step(xa, wa, [&](auto i_c) {
        constexpr int I = decltype(i_c)::value;
        rd1(xb, wb, xs1, wr0 + 4224u, wr1 + 4224u, i_c);
        if constexpr (I == 1 || I == 7 || I == 13) xpiece(xb2, poison, IC<I / 6>{});
        if constexpr (I == 4 || I == 10 || I == 16 || I == 19) { constexpr int U = I == 19 ? 3 : I / 6; dma_w<1024 * U>(slot + U * dm, voff, srd, so + U * ds); }
      });
      const unsigned d = tog ? 0u - 8448u : 8448u;
      wr0 += d; wr1 += d;
      wait_vm_ne<6, 11>(pp);
      asm volatile("s_barrier" ::: "memory");
      SBAR();
      step(xb, wb, [&](auto i_c) {
        constexpr int I = decltype(i_c)::value;
        rd1(xa, wa, xs0, wr0, wr1, i_c);
        if constexpr (I % 5 == 4) dma_w<1024 * (I / 5)>(slot + 4224u + (I / 5) * dm, voff, srd, so2 + (I / 5) * ds);
      });
      tog ^= 8448u;
      p0 = p1; p1 = p2;
    }
  }
  asm volatile("s_nop 7" ::: "memory");
  STAMP(2);
  barrier_lds();
#pragma unroll
  for (int u = 0; u < 2; ++u)
#pragma unroll
    for (int m = 0; m < 5; ++m) lds_wr16(lb + L_QX + ((2u * w + u) * 5u + m) * 1024u + lane * 16u, pack8(qa[u][0][m], qa[u][1][m]));
  barrier_lds();
  frag Q[8][5];
  {
    const unsigned qa0 = lb + L_QX + lane * 16u;
#define RDQ(s) lds_rd16_acc<(s * 5 + 0) * 1024>(Q[s][0], qa0); lds_rd16_acc<(s * 5 + 1) * 1024>(Q[s][1], qa0); lds_rd16_acc<(s * 5 + 2) * 1024>(Q[s][2], qa0); \
               lds_rd16_acc<(s * 5 + 3) * 1024>(Q[s][3], qa0); lds_rd16_acc<(s * 5 + 4) * 1024>(Q[s][4], qa0);
    RDQ(0) RDQ(1) RDQ(2) RDQ(3) RDQ(4) RDQ(5) RDQ(6) RDQ(7)
#undef RDQ
  }
#define QOPS(s) "+a"(Q[s][0]), "+a"(Q[s][1]), "+a"(Q[s][2]), "+a"(Q[s][3]), "+a"(Q[s][4])
  asm volatile("s_waitcnt lgkmcnt(0)\n\ts_barrier" : QOPS(0), QOPS(1), QOPS(2), QOPS(3) :: "memory");
  asm volatile("" : QOPS(4), QOPS(5), QOPS(6), QOPS(7) :: "memory");
#undef QOPS
#pragma unroll
  for (int c = 0; c < 4; ++c) dma4_v<4096>(vbase + c * 4096u, v_voff, sV, (32u * c) * 512u);

  const unsigned kr = kbase + (r16 >> 1) * 1056u + (r16 & 1u) * 512u + ((g ^ (r16 & 1u)) << 4);
  const unsigned fv = (2u * g + (q4 >> 1)) & 3u;
  unsigned vr[4];
#pragma unroll
  for (int c = 0; c < 4; ++c) vr[c] = vbase + (4u * g + q4) * 128u + ((((c ^ fv) << 1) | (p4 >> 1)) << 4) + (p4 & 1u) * 8u;
  unsigned pa = lb + L_P + lane * 16u, pw = pa + w * 5120u;
  unsigned ma = lb + L_MX + r16 * 4u;
  asm volatile("" : "+v"(pa), "+v"(pw), "+v"(ma), "+v"(vr[0]), "+v"(vr[1]), "+v"(vr[2]), "+v"(vr[3]));

  float mrow[5], lrow[5];
  frag P[5];
#pragma unroll
  for (int m = 0; m < 5; ++m) { mrow[m] = -__builtin_inff(); lrow[m] = 0.f; P[m] = frag{0u, 0u, 0u, 0u}; }

  frag ka[4], kb[4], pA[5], pB[5];
  u32x2 va[8], vb[8];
  auto rdK1 = [&](frag (&f)[4], int u, int half, int s) { f[s] = lds_rd16(kr + u * KSLOT + (4 * half + s) * 64u); };
  auto rdV1 = [&](u32x2 (&f)[8], int c, int i) { f[i] = lds_tr1(vr[i >> 1] + c * 4096u + (i & 1) * 2048u); };
  auto rdP1 = [&](frag (&f)[5], int c, int m) { f[m] = lds_rd16(pa + (c * 5 + m) * 1024u); };
  auto grpQK = [&](f32x4 (&S)[5], frag (&f)[4], auto half_c, auto fill) {
    constexpr int half = decltype(half_c)::value;
    for20([&](auto i_c) {
      constexpr int I = decltype(i_c)::value, s = I / 5, m = I % 5;
      if (half == 0 && s == 0) mm_va0(S[m], f[s], Q[s][m]); else mm_va(S[m], f[s], Q[4 * half + s][m]);
      fill(i_c); SBAR();
    });
  };
  auto grpPV = [&](u32x2 (&vf)[8], frag (&pf)[5], auto fill) {
    for20([&](auto i_c) {
      constexpr int I = decltype(i_c)::value, m = I / 4, d = I % 4;
      mm_av(O[d][m], __builtin_shufflevector(vf[2 * d], vf[2 * d + 1], 0, 1, 2, 3), pf[m]);
      fill(i_c); SBAR();
    });
  };
  auto kpiece = [&](int u, auto i_c, unsigned kso) {
    constexpr int I = decltype(i_c)::value;
    dma_kv<1024 * (I & 3)>(kbase + u * KSLOT + 32u * I + 4096u * (I >> 2), k_voff, sK, kso + u * 8192u + 4096u * (I >> 2));
  };
  auto vpiece = [&](int c, auto i_c, unsigned vso) {
    constexpr int I = decltype(i_c)::value;
    dma_kv<1024 * I>(vbase + c * 4096u, v_voff, sV, vso + c * 16384u + 3072u * I);
  };
  auto pv = [&](auto last_tag, unsigned vso) {
    constexpr bool LAST = decltype(last_tag)::value;
    auto fill = [&](u32x2 (&vn)[8], frag (&pn)[5], auto c_c) {
      return [&vn, &pn, &rdV1, &rdP1, &vpiece, vso](auto i_c) {
        constexpr int I = decltype(i_c)::value, C = decltype(c_c)::value;
        if constexpr (C < 3 && I < 8) rdV1(vn, C + 1, I);
        if constexpr (C < 3 && I >= 8 && I < 13) rdP1(pn, C + 1, I - 8);
        if constexpr (!LAST && C > 0 && I % 5 == 3) vpiece(C - 1, IC<I / 5>{}, vso);
      };
    };
#pragma unroll
    for (int m = 0; m < 5; ++m) rdP1(pA, 0, m);
    if (LAST) wait_vm<8>(); else wait_vm<24>();
    SBAR(); grpPV(va, pA, fill(vb, pB, IC<0>{}));
    if (LAST) wait_vm<4>(); else wait_vm<20>();
    grpPV(vb, pB, fill(va, pA, IC<1>{}));
    if (LAST) wait_vm<0>(); else wait_vm<20>();
    grpPV(va, pA, fill(vb, pB, IC<2>{}));
    grpPV(vb, pB, fill(va, pA, IC<3>{}));
  };

  wait_vm<24>();
#pragma unroll
  for (int s = 0; s < 4; ++s) rdK1(ka, 0, 0, s);
#pragma unroll 1
  for (unsigned j = 0; j < 8u; ++j) {
    f32x4 S0[5], S1[5];
    float mx[5];
    STAMP(3 + 2 * j);
    const unsigned kso = (128u * (j + 1u) + 32u * w) * 512u;
    SBAR();
    grpQK(S0, ka, IC<0>{}, [&](auto i_c) {
      constexpr int I = decltype(i_c)::value;
      if constexpr (I < 4) rdK1(kb, 0, 1, I);
      if constexpr (I >= 5 && I % 3 == 2) lds_wr16(pw + ((I - 5) / 3) * 1024u, P[(I - 5) / 3]);
    });
    wait_vm_eq<1, 0, 16>(j);
    grpQK(S0, kb, IC<1>{}, [&](auto i_c) {
      constexpr int I = decltype(i_c)::value;
      if constexpr (I < 4) rdK1(ka, 1, 0, I);
      if constexpr (I == 4) asm volatile("s_waitcnt lgkmcnt(4)" ::: "memory");
      if constexpr (I % 5 == 4) kpiece(0, IC<I / 5>{}, kso);
    });
    grpQK(S1, ka, IC<0>{}, [&](auto i_c) {
      constexpr int I = decltype(i_c)::value;
      if constexpr (I < 4) rdK1(kb, 1, 1, I);
      if constexpr (I % 5 == 4) kpiece(0, IC<I / 5 + 4>{}, kso);
      if constexpr (I >= 10 && I < 15) mx[I - 10] = max3f(S0[I - 10][0], S0[I - 10][1], S0[I - 10][2]);
      if constexpr (I >= 15) mx[I - 15] = max2f(mx[I - 15], S0[I - 15][3]);
    });
    asm volatile("s_waitcnt lgkmcnt(0)" ::: "memory");
    wait_vm<20>();
    grpQK(S1, kb, IC<1>{}, [&](auto i_c) {
      constexpr int I = decltype(i_c)::value;
      if constexpr (I < 8) rdV1(va, 0, I);
      if constexpr (I % 5 == 4) kpiece(1, IC<I / 5>{}, kso);
    });
    if (j == 3u) STAMP(24);
    kpiece(1, IC<4>{}, kso); kpiece(1, IC<5>{}, kso);
    asm volatile("s_nop 1" ::: "memory");
    SBAR();
#pragma unroll
    for (int m = 0; m < 5; ++m) mx[m] = max3f(mx[m], S1[m][0], S1[m][1]);
#pragma unroll
    for (int m = 0; m < 5; ++m) mx[m] = max3f(mx[m], S1[m][2], S1[m][3]);
    SBAR();
    lds_max<0>(ma, mx[0]); lds_max<64>(ma, mx[1]); lds_max<128>(ma, mx[2]); lds_max<192>(ma, mx[3]); lds_max<256>(ma, mx[4]);
    kpiece(1, IC<6>{}, kso); kpiece(1, IC<7>{}, kso);
    if (j == 3u) STAMP(25);
    barrier_lds();
    STAMP(4 + 2 * j);
    float cm[5];
#pragma unroll
    for (int m = 0; m < 5; ++m) cm[m] = lds_rdf(ma + m * 64u);
    if (j > 0u) pv(FF{}, j * 65536u);
    if (j == 3u) STAMP(26);
    float al[5];
    float gr = max3f(cm[0] - mrow[0], cm[1] - mrow[1], cm[2] - mrow[2]);
    gr = max3f(gr, cm[3] - mrow[3], cm[4] - mrow[4]);
#pragma unroll
    for (int m = 0; m < 5; ++m) al[m] = 1.f;
    if (__builtin_expect(__any(gr > RESCALE_THR / SM_C), 0)) {
#pragma unroll
      for (int m = 0; m < 5; ++m) { al[m] = __builtin_amdgcn_exp2f((mrow[m] - cm[m]) * SM_C); mrow[m] = cm[m]; }
      if (j > 0u) {
        asm volatile("s_nop 7" ::: "memory");
#pragma unroll
        for (int d = 0; d < 4; ++d)
#pragma unroll
          for (int m = 0; m < 5; ++m) { asm volatile("" : "+a"(O[d][m])); O[d][m] *= al[m]; asm volatile("" : "+a"(O[d][m])); }
      }
    }
    {
      const unsigned vso = j * 65536u;
      float nm[5];
#pragma unroll
      for (int m = 0; m < 5; ++m) nm[m] = -mrow[m] * SM_C;
#pragma unroll
      for (int r = 0; r < 4; ++r)
#pragma unroll
        for (int m = 0; m < 5; ++m) { S0[m][r] = fmaf(S0[m][r], SM_C, nm[m]); S1[m][r] = fmaf(S1[m][r], SM_C, nm[m]); }
      SBAR(); dma_kv_if<0>(j, vbase + 12288u, v_voff, sV, vso + 49152u); SBAR();
#pragma unroll
      for (int m = 0; m < 5; ++m) { S0[m][0] = __builtin_amdgcn_exp2f(S0[m][0]); S0[m][1] = __builtin_amdgcn_exp2f(S0[m][1]); }
      SBAR(); dma_kv_if<1024>(j, vbase + 12288u, v_voff, sV, vso + 49152u + 3072u); SBAR();
#pragma unroll
      for (int m = 0; m < 5; ++m) { S0[m][2] = __builtin_amdgcn_exp2f(S0[m][2]); S0[m][3] = __builtin_amdgcn_exp2f(S0[m][3]); }
      SBAR(); dma_kv_if<2048>(j, vbase + 12288u, v_voff, sV, vso + 49152u + 6144u); SBAR();
#pragma unroll
      for (int m = 0; m < 5; ++m) { S1[m][0] = __builtin_amdgcn_exp2f(S1[m][0]); S1[m][1] = __builtin_amdgcn_exp2f(S1[m][1]); }
      SBAR(); dma_kv_if<3072>(j, vbase + 12288u, v_voff, sV, vso + 49152u + 9216u); SBAR();
#pragma unroll
      for (int m = 0; m < 5; ++m) { S1[m][2] = __builtin_amdgcn_exp2f(S1[m][2]); S1[m][3] = __builtin_amdgcn_exp2f(S1[m][3]); }
      SBAR();
      wait_vm_eq<0, 8, 24>(j);
#pragma unroll
      for (int s = 0; s < 4; ++s) rdK1(ka, 0, 0, s);
      SBAR();
      float t0[5], t1[5];
#pragma unroll
      for (int m = 0; m < 5; ++m) { t0[m] = addf(S0[m][0], S0[m][1]); t1[m] = addf(S0[m][2], S0[m][3]); }
#pragma unroll
      for (int m = 0; m < 5; ++m) { t0[m] = addf(t0[m], S1[m][0]); t1[m] = addf(t1[m], S1[m][1]); }
#pragma unroll
      for (int m = 0; m < 5; ++m) { t0[m] = addf(t0[m], S1[m][2]); t1[m] = addf(t1[m], S1[m][3]); }
#pragma unroll
      for (int m = 0; m < 5; ++m) P[m] = pack8(S0[m], S1[m]);
#pragma unroll
      for (int m = 0; m < 5; ++m) lrow[m] = fmaf(lrow[m], al[m], addf(t0[m], t1[m]));
      SBAR();
    }
    if (j == 3u) STAMP(27);
    asm volatile("s_barrier" ::: "memory");
  }
  STAMP(19);
  const unsigned sa = lb + L_ST + r16 * 16u;
#pragma unroll
  for (int m = 0; m < 5; ++m) lds_wr16(pw + m * 1024u, P[m]);
#pragma unroll
  for (int m = 0; m < 5; ++m) lds_wrf(sa + m * 256u + w * 4u, xsum_g(lrow[m]));
  wait_vm<12>();
#pragma unroll
  for (int i = 0; i < 8; ++i) rdV1(va, 0, i);
  barrier_lds();
  STAMP(20);
  f32x4 st[5];
#pragma unroll
  for (int m = 0; m < 5; ++m) st[m] = lds_rdf4(sa + m * 256u);
  pv(TT{}, 0u);
  float inv[5];
#pragma unroll
  for (int m = 0; m < 5; ++m) inv[m] = 1.0f / ((st[m][0] + st[m][1]) + (st[m][2] + st[m][3]));
  asm volatile("s_nop 7" ::: "memory");
  STAMP(21);
  char* op = ows + (size_t)((br * NB + b) * HQ + h) * 40960u + lane * 16u;
#pragma unroll
  for (int m = 0; m < 5; ++m)
#pragma unroll
    for (int u = 0; u < 2; ++u) {
      asm volatile("" : "+a"(O[2 * u][m]), "+a"(O[2 * u + 1][m]));
      *(frag*)(op + ((2u * w + u) * 5u + m) * 1024u) = pack8(O[2 * u][m] * inv[m], O[2 * u + 1][m] * inv[m]);
    }
  STAMP(22);
  STAMP_FLUSH();
}

__global__ __launch_bounds__(256, 1) void oproj_k2(const char* __restrict__ woA, const char* __restrict__ woB, const char* __restrict__ ows,
                                                   float* __restrict__ part) {
  __shared__ __attribute__((aligned(16))) char lds_mem[143360];
  const unsigned lb = (unsigned)(size_t)(LDS3 char*)lds_mem;
  const unsigned tid = threadIdx.x, lane = tid & 63u;
  const unsigned w = __builtin_amdgcn_readfirstlane(tid >> 6);
  const unsigned bid = blockIdx.x, xg = bid & 7u, j = bid >> 3;
  const unsigned br = xg >> 2, kq = xg & 3u, b = j >> 2, nq = j & 3u, h = 4u * kq + w;
  const unsigned r16 = lane & 15u, g = lane >> 4, q4 = r16 >> 2, p4 = lane & 3u;
  const srd_t sW = mk_srd((br ? woB : woA) + nq * 224u, HQ * DH * DM * 2 - nq * 224u);
  const unsigned ring = lb + w * 35840u;
  frag of[8][5];
  const char* op = ows + (size_t)((br * NB + b) * HQ + h) * 40960u + lane * 16u;
#pragma unroll
  for (int s = 0; s < 8; ++s)
#pragma unroll
    for (int m = 0; m < 5; ++m) of[s][m] = *(const frag*)(op + (s * 5 + m) * 1024u);
  unsigned voff[7];
#pragma unroll
  for (int i = 0; i < 7; ++i) { const unsigned n = i * 64u + lane, row = n / 14u, ch = n - row * 14u; voff[i] = row * 896u + ch * 16u; }
  auto issue = [&](int s) {
#pragma unroll
    for (int i = 0; i < 7; ++i) dma1(ring + (s % 5) * 7168u + i * 1024u, voff[i], sW, (h * 256u + 32u * s) * 896u);
  };
#pragma unroll
  for (int s = 0; s < 5; ++s) issue(s);
  f32x4 acc[7][5];
#pragma unroll
  for (int n = 0; n < 7; ++n)
#pragma unroll
    for (int m = 0; m < 5; ++m) acc[n][m] = f32x4{0.f, 0.f, 0.f, 0.f};
  const unsigned ar = ring + (4u * g + q4) * 224u + p4 * 8u;
#pragma unroll
  for (int s = 0; s < 8; ++s) {
    if (s <= 3) wait_vm<28>(); else if (s == 4) wait_vm<21>(); else if (s == 5) wait_vm<14>(); else if (s == 6) wait_vm<7>(); else wait_vm<0>();
    frag wf[7];
#pragma unroll
    for (int n = 0; n < 7; ++n) { const unsigned a = ar + (s % 5) * 7168u + n * 32u; wf[n] = lds_tr2(a, a + 3584u); }
#pragma unroll
    for (int n = 0; n < 7; ++n)
#pragma unroll
      for (int m = 0; m < 5; ++m) acc[n][m] = MFMA16(__builtin_bit_cast(bf16x8, wf[n]), __builtin_bit_cast(bf16x8, of[s][m]), acc[n][m]);
    SBAR();
    if (s + 5 < 8) issue(s + 5);
    SBAR();
  }
#pragma unroll
  for (int n = 0; n < 7; ++n)
#pragma unroll
    for (int m = 0; m < 5; ++m) lds_wrf4(ring + (n * 5 + m) * 1024u + lane * 16u, acc[n][m]);
  barrier_lds();
  f32x4* dst = (f32x4*)part + (size_t)((((kq * 2u + br) * NB + b) * 4u + nq) * 2240u);
#pragma unroll
  for (int i = 0; i < 9; ++i) {
    const unsigned e = i * 256u + tid;
    if (e < 2240u) {
      const unsigned a = lb + e * 16u;
      dst[e] = (lds_rdf4(a) + lds_rdf4(a + 35840u)) + (lds_rdf4(a + 71680u) + lds_rdf4(a + 107520u));
    }
  }
}

__global__ __launch_bounds__(256) void reduce_k3(const float* __restrict__ part, char* __restrict__ out) {
  const unsigned e = blockIdx.x * 256u + threadIdx.x;
  const f32x4* p = (const f32x4*)part + e;
  const f32x4 v = (p[0] + p[143360]) + (p[2 * 143360] + p[3 * 143360]);
  const unsigned grp = e / 2240u, i = e - grp * 2240u, tile = i >> 6, lane = i & 63u;
  const unsigned nq = grp & 3u, b = (grp >> 2) & 7u, br = grp >> 5, n = tile / 5u, m = tile - n * 5u;
  const unsigned row = b * SQ + 16u * m + (lane & 15u), col = br * DM + nq * 112u + 16u * n + 4u * (lane >> 4);
  *(u32x2*)(out + ((size_t)row * (2 * DM) + col) * 2) = u32x2{cvtpk(v[0], v[1]), cvtpk(v[2], v[3])};
}

extern "C" void kernel_launch(void* const* d_in, const int* in_sizes, int n_in, void* d_out, int out_size, void* d_ws, size_t ws_size,
                              hipStream_t stream) {
  char* ows = (char*)d_ws;
  float* part = (float*)((char*)d_ws + 256u * 40960u);
  attn_k1<<<256, 256, 0, stream>>>((const char*)d_in[0], (const char*)d_in[1], (const char*)d_in[2], (const char*)d_in[3],
                                   (const char*)d_in[5], (const char*)d_in[6], (const char*)d_in[7], (const char*)d_in[8], ows);
  oproj_k2<<<256, 256, 0, stream>>>((const char*)d_in[4], (const char*)d_in[9], ows, part);
  reduce_k3<<<560, 256, 0, stream>>>(part, (char*)d_out);
}
```

```cpp
#include <hip/hip_runtime.h>
#include <hip/hip_bf16.h>
#include <utility>

typedef float f32x4 __attribute__((ext_vector_type(4)));
typedef float f32x2 __attribute__((ext_vector_type(2)));
typedef __bf16 bf16x8 __attribute__((ext_vector_type(8)));
typedef __bf16 bf16x4 __attribute__((ext_vector_type(4)));
typedef __bf16 bf16x2 __attribute__((ext_vector_type(2)));
typedef unsigned u32x4 __attribute__((ext_vector_type(4)));
typedef unsigned u32x2 __attribute__((ext_vector_type(2)));
typedef __amdgpu_buffer_rsrc_t srd_t;
#define DI __device__ __forceinline__
#define LDS3 __attribute__((address_space(3)))
#define SBAR() __builtin_amdgcn_sched_barrier(0)
#define MFMA16(a, b, c) __builtin_amdgcn_mfma_f32_16x16x32_bf16((a), (b), (c), 0, 0, 0)

#define KV_POL " nt"
#define W_POL " nt"
#define RESCALE_THR 8.0f

#define STAMP(i) do {} while (0)
#define STAMP_FLUSH() do {} while (0)

constexpr int NB = 8, SQ = 80, DM = 448, HQ = 16, HKV = 8, SK = 1024, DH = 256;
constexpr float SM_C = 0.0625f * 1.4426950408889634f;

constexpr unsigned KSLOT = 8448;
constexpr unsigned L_K = 0;
constexpr unsigned L_V = 4 * 2 * KSLOT;
constexpr unsigned L_P = L_V + 65536;
constexpr unsigned L_ST = L_P + 20480;
constexpr unsigned L_X = L_V;
constexpr unsigned L_MX = L_ST + 1280;
constexpr unsigned L_SCR = L_MX + 512;
constexpr unsigned L_QX = L_V;
constexpr unsigned LDS1 = 163840;
constexpr int CODE_PF = 6;
constexpr unsigned PART_B = 71680;
constexpr unsigned WS_FLAG = 24u << 20, WS_ACK = WS_FLAG + 65536u;
constexpr unsigned long long HO_STEP = 0x9E3779B97F4A7C15ull;
#define PART_AUX 16

constexpr unsigned wq_slot(int s) { return ((s + 2) & 3) * 4224u; }

DI unsigned cvtpk(float lo, float hi) { f32x2 v = {lo, hi}; bf16x2 b = __builtin_convertvector(v, bf16x2); return __builtin_bit_cast(unsigned, b); }
typedef u32x4 frag;
DI frag pack8(const f32x4& a, const f32x4& b) { return u32x4{cvtpk(a[0], a[1]), cvtpk(a[2], a[3]), cvtpk(b[0], b[1]), cvtpk(b[2], b[3])}; }
DI frag lds_rd16(unsigned a) { return *(const LDS3 frag*)(size_t)a; }
DI f32x4 lds_rdf4(unsigned a) { return *(const LDS3 f32x4*)(size_t)a; }
DI void lds_wr16(unsigned a, frag v) { *(LDS3 frag*)(size_t)a = v; }
DI void lds_wrf4(unsigned a, f32x4 v) { *(LDS3 f32x4*)(size_t)a = v; }
DI void lds_wrf(unsigned a, float v) { *(LDS3 float*)(size_t)a = v; }
DI frag lds_tr2(unsigned a0, unsigned a1) {
  const u32x2 lo = __builtin_bit_cast(u32x2, __builtin_amdgcn_ds_read_tr16_b64_v4bf16((LDS3 bf16x4*)(size_t)a0));
  const u32x2 hi = __builtin_bit_cast(u32x2, __builtin_amdgcn_ds_read_tr16_b64_v4bf16((LDS3 bf16x4*)(size_t)a1));
  return __builtin_shufflevector(lo, hi, 0, 1, 2, 3);
}
DI u32x2 lds_tr1(unsigned a) { return __builtin_bit_cast(u32x2, __builtin_amdgcn_ds_read_tr16_b64_v4bf16((LDS3 bf16x4*)(size_t)a)); }
DI float lds_rdf(unsigned a) { return *(const LDS3 float*)(size_t)a; }
template <int OFF> DI void lds_max(unsigned a, float v) { asm volatile("ds_max_f32 %0, %1 offset:%2" ::"v"(a), "v"(v), "i"(OFF) : "memory"); }
DI srd_t mk_srd(const void* p, unsigned bytes) { return __builtin_amdgcn_make_buffer_rsrc((void*)p, 0, bytes, 0x00020000); }
template <int N> DI void wait_vm() { asm volatile("s_waitcnt vmcnt(%0)" ::"i"(N) : "memory"); }
DI void barrier_lds() { asm volatile("s_waitcnt lgkmcnt(0)\n\ts_barrier" ::: "memory"); }
DI float xmax_g(float x) {
  auto a = __builtin_amdgcn_permlane16_swap(__float_as_uint(x), __float_as_uint(x), false, false);
  x = fmaxf(__uint_as_float(a[0]), __uint_as_float(a[1]));
  auto b = __builtin_amdgcn_permlane32_swap(__float_as_uint(x), __float_as_uint(x), false, false);
  return fmaxf(__uint_as_float(b[0]), __uint_as_float(b[1]));
}
DI float xsum_g(float x) {
  auto a = __builtin_amdgcn_permlane16_swap(__float_as_uint(x), __float_as_uint(x), false, false);
  x = __uint_as_float(a[0]) + __uint_as_float(a[1]);
  auto b = __builtin_amdgcn_permlane32_swap(__float_as_uint(x), __float_as_uint(x), false, false);
  return __uint_as_float(b[0]) + __uint_as_float(b[1]);
}

DI void dma1(unsigned lds, unsigned voff, srd_t srd, unsigned soff) {
  asm volatile("s_mov_b32 m0, %0\n\ts_nop 0\n\tbuffer_load_dwordx4 %1, %2, %3 offen" W_POL " lds" ::"s"(lds), "v"(voff), "s"(srd), "s"(soff) : "memory");
}
DI void dma_k(unsigned lds, unsigned voff, srd_t srd, unsigned soff) {
  asm volatile(
      "s_mov_b32 m0, %0\n\ts_nop 0\n\tbuffer_load_dwordx4 %1, %2, %3 offen" KV_POL " lds\n\t"
      "s_add_u32 m0, m0, 32\n\ts_nop 0\n\tbuffer_load_dwordx4 %1, %2, %3 offen offset:1024" KV_POL " lds\n\t"
      "s_add_u32 m0, m0, 32\n\ts_nop 0\n\tbuffer_load_dwordx4 %1, %2, %3 offen offset:2048" KV_POL " lds\n\t"
      "s_add_u32 m0, m0, 32\n\ts_nop 0\n\tbuffer_load_dwordx4 %1, %2, %3 offen offset:3072" KV_POL " lds\n\t"
      "s_add_u32 m0, m0, 4128\n\ts_nop 0\n\tbuffer_load_dwordx4 %1, %2, %4 offen" KV_POL " lds\n\t"
      "s_add_u32 m0, m0, 32\n\ts_nop 0\n\tbuffer_load_dwordx4 %1, %2, %4 offen offset:1024" KV_POL " lds\n\t"
      "s_add_u32 m0, m0, 32\n\ts_nop 0\n\tbuffer_load_dwordx4 %1, %2, %4 offen offset:2048" KV_POL " lds\n\t"
      "s_add_u32 m0, m0, 32\n\ts_nop 0\n\tbuffer_load_dwordx4 %1, %2, %4 offen offset:3072" KV_POL " lds"
      ::"s"(lds), "v"(voff), "s"(srd), "s"(soff), "s"(soff + 4096u) : "memory", "scc");
}
#define DMA4(NAME, POL)                                                                                                   \
  template <unsigned STEP> DI void NAME(unsigned lds, unsigned voff, srd_t srd, unsigned soff) {                          \
    asm volatile("s_mov_b32 m0, %0\n\ts_nop 0\n\tbuffer_load_dwordx4 %1, %2, %3 offen" POL " lds\n\t"                     \
                 "buffer_load_dwordx4 %1, %2, %4 offen offset:1024" POL " lds\n\t"                                        \
                 "buffer_load_dwordx4 %1, %2, %5 offen offset:2048" POL " lds\n\t"                                        \
                 "buffer_load_dwordx4 %1, %2, %6 offen offset:3072" POL " lds"                                            \
                 ::"s"(lds), "v"(voff), "s"(srd), "s"(soff), "s"(soff + (STEP - 1024u)), "s"(soff + 2u * (STEP - 1024u)), \
                 "s"(soff + 3u * (STEP - 1024u)) : "memory");                                                             \
  }
DMA4(dma4_v, KV_POL)
DMA4(dma4_w, W_POL)

template <int IMM> DI void dma_kv(unsigned lds, unsigned voff, srd_t srd, unsigned soff) {
  asm volatile("s_mov_b32 m0, %0\n\ts_nop 0\n\tbuffer_load_dwordx4 %1, %2, %3 offen offset:%4" KV_POL " lds" ::"s"(lds), "v"(voff), "s"(srd), "s"(soff), "i"(IMM) : "memory");
}
template <int IMM> DI void dma_w(unsigned lds, unsigned voff, srd_t srd, unsigned soff) {
  asm volatile("s_mov_b32 m0, %0\n\ts_nop 0\n\tbuffer_load_dwordx4 %1, %2, %3 offen offset:%4" W_POL " lds" ::"s"(lds), "v"(voff), "s"(srd), "s"(soff), "i"(IMM) : "memory");
}
template <int K, int NA, int NB> DI void wait_vm_eq(unsigned v) {
  asm volatile("s_cmp_lg_u32 %0, %3\n\ts_cbranch_scc1 1f\n\ts_waitcnt vmcnt(%1)\n1:\n\ts_waitcnt vmcnt(%2)" ::"s"(v), "i"(NA), "i"(NB), "i"(K) : "memory", "scc");
}
template <int K, int NA> DI void wait_vm_ne(unsigned v) {
  asm volatile("s_cmp_eq_u32 %0, %2\n\ts_cbranch_scc1 1f\n\ts_waitcnt vmcnt(%1)\n1:" ::"s"(v), "i"(NA), "i"(K) : "memory", "scc");
}
template <int IMM> DI void dma_kv_if(unsigned c, unsigned lds, unsigned voff, srd_t srd, unsigned soff) {
  asm volatile("s_cmp_eq_u32 %5, 0\n\ts_cbranch_scc1 1f\n\ts_mov_b32 m0, %0\n\ts_nop 0\n\tbuffer_load_dwordx4 %1, %2, %3 offen offset:%4" KV_POL " lds\n1:"
               ::"s"(lds), "v"(voff), "s"(srd), "s"(soff), "i"(IMM), "s"(c) : "memory", "scc");
}
#define O_POL ""
DI void dma_o(unsigned lds, unsigned voff, srd_t srd, unsigned soff) {
  asm volatile("s_mov_b32 m0, %0\n\ts_nop 0\n\tbuffer_load_dwordx4 %1, %2, %3 offen" O_POL " lds" ::"s"(lds), "v"(voff), "s"(srd), "s"(soff) : "memory");
}
typedef __attribute__((address_space(1))) unsigned long long gu64;
#define RLX_AGENT __ATOMIC_RELAXED, __HIP_MEMORY_SCOPE_AGENT
template <int I> struct IC { static constexpr int value = I; };
template <class F, int... Is> DI void for_seq(F&& f, std::integer_sequence<int, Is...>) { (f(IC<Is>{}), ...); }
template <class F> DI void for20(F&& f) { for_seq(f, std::make_integer_sequence<int, 20>{}); }
DI float max3f(float a, float b, float c) { float r; asm("v_max3_f32 %0, %1, %2, %3" : "=v"(r) : "v"(a), "v"(b), "v"(c)); return r; }
DI float max2f(float a, float b) { float r; asm("v_max_f32_e32 %0, %1, %2" : "=v"(r) : "v"(a), "v"(b)); return r; }
DI float addf(float a, float b) { float r; asm("v_add_f32_e32 %0, %1, %2" : "=v"(r) : "v"(a), "v"(b)); return r; }
struct TT { static constexpr bool value = true; };
struct FF { static constexpr bool value = false; };
DI void mm_va0(f32x4& d, const frag& a, const frag& bq) { asm volatile("v_mfma_f32_16x16x32_bf16 %0, %1, %2, 0" : "=&v"(d) : "v"(a), "a"(bq)); }
DI void mm_va(f32x4& d, const frag& a, const frag& bq) { asm volatile("v_mfma_f32_16x16x32_bf16 %0, %1, %2, %0" : "+v"(d) : "v"(a), "a"(bq)); }
DI void mm_av(f32x4& d, const frag& a, const frag& b) { asm volatile("v_mfma_f32_16x16x32_bf16 %0, %1, %2, %0" : "+a"(d) : "v"(a), "v"(b)); }
DI void mm_vv(f32x4& d, const frag& a, const frag& b) { asm volatile("v_mfma_f32_16x16x32_bf16 %0, %1, %2, %0" : "+v"(d) : "v"(a), "v"(b)); }
template <int OFF> DI void lds_rd16_acc(frag& d, unsigned a) { asm volatile("ds_read_b128 %0, %1 offset:%2" : "=a"(d) : "v"(a), "i"(OFF) : "memory"); }

__global__ __launch_bounds__(256, 1) void gqa_block_fused(const char* __restrict__ xA, const char* __restrict__ kA, const char* __restrict__ vA,
                                                          const char* __restrict__ wqA, const char* __restrict__ woA, const char* __restrict__ xB,
                                                          const char* __restrict__ kB, const char* __restrict__ vB, const char* __restrict__ wqB,
                                                          const char* __restrict__ woB, char* ws, char* __restrict__ out) {
  __shared__ __attribute__((aligned(16))) char lds_mem[LDS1];
  const unsigned lb = (unsigned)(size_t)(LDS3 char*)lds_mem;
  const unsigned tid = threadIdx.x, lane = tid & 63u;
  const unsigned w = __builtin_amdgcn_readfirstlane(tid >> 6);
  const unsigned bid = blockIdx.x;
  asm volatile("" ::"s"(xA), "s"(kA), "s"(vA), "s"(wqA), "s"(woA), "s"(xB), "s"(kB), "s"(vB), "s"(wqB), "s"(woB), "s"(ws), "s"(out));
#define MAP 0
  const unsigned idx = bid >> 3, xg = bid & 7u;
  const unsigned combo = 2u * xg + (idx >> 4), br = combo >> 3, kvh = combo & 7u, b = (idx & 15u) >> 1, rsel = b;
  const unsigned h = 2u * kvh + (idx & 1u);
  const unsigned r16 = lane & 15u, g = lane >> 4, q4 = r16 >> 2, p4 = lane & 3u;

  const srd_t sX = mk_srd((br ? xB : xA) + (size_t)b * (SQ * DM * 2), SQ * DM * 2);
  const srd_t sW = mk_srd((br ? wqB : wqA) + h * (DH * 2), DM * HQ * DH * 2 - h * (DH * 2));
  const srd_t sK = mk_srd((br ? kB : kA) + (size_t)(b * HKV + kvh) * (SK * DH * 2), SK * DH * 2);
  const srd_t sV = mk_srd((br ? vB : vA) + (size_t)(b * HKV + kvh) * (SK * DH * 2), SK * DH * 2);
  STAMP(0);
  const unsigned grp = br * NB + b;
  char* const flagp = ws + WS_FLAG + grp * 128u;
  char* const ackp = ws + WS_ACK + grp * 2048u;
  unsigned long long tag;
  asm volatile("global_load_dwordx2 %0, %1, %2 sc1" : "=v"(tag) : "v"(r16 * 8u), "s"(flagp) : "memory");
  const unsigned kbase = lb + L_K + w * 2u * KSLOT;
  const unsigned vbase = lb + L_V + w * 16384u;
  const unsigned rot = 0;
  const unsigned wq_voff = (lane >> 3) * 8192u + (((lane & 7u) ^ (((lane >> 4) & 1u) << 2)) << 4) + w * 128u;
  const unsigned k_voff = (lane >> 5) * 512u + (((lane & 31u) ^ (lane >> 5)) << 4);
  const unsigned v_voff = (lane >> 3) * 512u + (((lane & 7u) ^ (((lane >> 4) & 3u) << 1)) << 4) + w * 128u;
  const unsigned x_voff = (lane >> 3) * 896u + (((lane & 7u) ^ ((4u * (w & 1u) + (lane >> 4)) & 7u)) << 4);
  const unsigned x2l = w < 2u ? lb + L_X + (8u + w) * 1024u : lb + L_SCR, x2m = w < 2u ? 10240u : 0u, x2s = w < 2u ? (8u + w) * 7168u : 0x40000000u;
  auto xpiece = [&](unsigned pb, unsigned poison, auto t_c) {
    constexpr unsigned T = decltype(t_c)::value;
    if constexpr (T < 2) dma_w<0>(lb + L_X + pb * 10240u + (4u * T + w) * 1024u, x_voff, sX, (4u * T + w) * 7168u + pb * 128u + poison);
    else dma_w<0>(x2l + pb * x2m, x_voff, sX, x2s + pb * 128u + poison);
  };
  auto xblock = [&](unsigned pb, unsigned poison) { xpiece(pb, poison, IC<0>{}); xpiece(pb, poison, IC<1>{}); xpiece(pb, poison, IC<2>{}); };
  unsigned p0 = rot, p1 = rot == 6u ? 0u : rot + 1u;
  xblock(p0, 0u);
  dma4_w<65536>(kbase + wq_slot(0), wq_voff, sW, p0 * 524288u);
  dma4_w<65536>(kbase + wq_slot(1), wq_voff, sW, p0 * 524288u + 262144u);
  xblock(p1, 0u);
  dma4_w<65536>(kbase + wq_slot(2), wq_voff, sW, p1 * 524288u);
  dma4_w<65536>(kbase + wq_slot(3), wq_voff, sW, p1 * 524288u + 262144u);
  f32x4 qa[2][2][5];
#pragma unroll
  for (int u = 0; u < 2; ++u)
#pragma unroll
    for (int t = 0; t < 2; ++t)
#pragma unroll
      for (int m = 0; m < 5; ++m) { qa[u][t][m] = f32x4{0.f, 0.f, 0.f, 0.f}; asm volatile("" : "+v"(qa[u][t][m])); }
  f32x4 O[4][5];
#pragma unroll
  for (int c = 0; c < 4; ++c)
#pragma unroll
    for (int m = 0; m < 5; ++m) { O[c][m] = f32x4{0.f, 0.f, 0.f, 0.f}; asm volatile("" : "+a"(O[c][m])); }
  {
    unsigned xr0 = lb + L_X + r16 * 128u + ((g ^ (r16 >> 1)) << 4), xr1 = xr0 ^ 64u;
    unsigned wr0 = kbase + 8448u + (8u * g + q4) * 128u + ((4u * (0u ^ (q4 >> 1)) + p4) << 4);
    unsigned wr1 = kbase + 8448u + (8u * g + q4) * 128u + ((4u * (1u ^ (q4 >> 1)) + p4) << 4);
    frag xa[5], xb[5];
    u32x2 wa[8], wb[8];
    auto rd1 = [&](frag (&xf)[5], u32x2 (&wf)[8], unsigned xs, unsigned a0, unsigned a1, auto i_c) {
      constexpr int I = decltype(i_c)::value;
      if constexpr (I < 8) wf[I] = lds_tr1(((I >> 2) ? a1 : a0) + ((I >> 1) & 1) * 8u + (I & 1) * 512u);
      else if constexpr (I < 13) xf[I - 8] = lds_rd16(xs + (I - 8) * 2048u);
    };
    auto step = [&](frag (&xf)[5], u32x2 (&wf)[8], auto fill) {
      for20([&](auto i_c) {
        constexpr int I = decltype(i_c)::value, m = I / 4, ut = I % 4;
        mm_vv(qa[ut >> 1][ut & 1][m], __builtin_shufflevector(wf[2 * ut], wf[2 * ut + 1], 0, 1, 2, 3), xf[m]);
        fill(i_c); SBAR();
      });
    };
    if (tid < 80u) lds_wrf(lb + L_MX + tid * 4u, -__builtin_inff());
    STAMP(23);
    wait_vm<15>();
    asm volatile("" : "+v"(tag));
    tag += HO_STEP;
    if (tid < 16u) asm volatile("global_store_dwordx2 %0, %1, %2 sc1" ::"v"(h * 128u + tid * 8u), "v"(tag), "s"(ackp) : "memory");
    barrier_lds();
    STAMP(1);
    for20([&](auto i_c) { rd1(xa, wa, xr0 + p0 * 10240u, wr0, wr1, i_c); });
    unsigned tog = 8448u;
#pragma unroll 1
    for (unsigned pp = 0; pp < 7u; ++pp) {
      if (pp == 1u) STAMP(28); if (pp == 3u) STAMP(29); if (pp == 5u) STAMP(30);
      const bool more = pp < 5u;
      const srd_t srd = more ? sW : sK;
      const unsigned voff = more ? wq_voff : k_voff, dm = more ? 0u : 32u, ds = more ? 64512u : 0u;
      const unsigned p2 = p1 == 6u ? 0u : p1 + 1u;
      const unsigned so = more ? p2 * 524288u : (32u * w) * 512u + (pp - 5u) * 8192u, so2 = so + (more ? 262144u : 4096u);
      const unsigned xb2 = more ? p2 : 7u, poison = more ? 0u : 0x40000000u;
      const unsigned xs1 = xr1 + p0 * 10240u, xs0 = xr0 + p1 * 10240u, slot = kbase + tog;
      wait_vm<11>();
      SBAR();
      step(xa, wa, [&](auto i_c) {
        constexpr int I = decltype(i_c)::value;
        rd1(xb, wb, xs1, wr0 + 4224u, wr1 + 4224u, i_c);
        if constexpr (I == 1 || I == 7 || I == 13) xpiece(xb2, poison, IC<I / 6>{});
        if constexpr (I == 4 || I == 10 || I == 16 || I == 19) { constexpr int U = I == 19 ? 3 : I / 6; dma_w<1024 * U>(slot + U * dm, voff, srd, so + U * ds); }
      });
      const unsigned d = tog ? 0u - 8448u : 8448u;
      wr0 += d; wr1 += d;
      wait_vm_ne<6, 11>(pp);
      asm volatile("s_barrier" ::: "memory");
      SBAR();
      step(xb, wb, [&](auto i_c) {
        constexpr int I = decltype(i_c)::value;
        rd1(xa, wa, xs0, wr0, wr1, i_c);
        if constexpr (I % 5 == 4) dma_w<1024 * (I / 5)>(slot + 4224u + (I / 5) * dm, voff, srd, so2 + (I / 5) * ds);
      });
      tog ^= 8448u;
      p0 = p1; p1 = p2;
    }
  }
  asm volatile("s_nop 7" ::: "memory");
  STAMP(2);
  barrier_lds();
#pragma unroll
  for (int u = 0; u < 2; ++u)
#pragma unroll
    for (int m = 0; m < 5; ++m) lds_wr16(lb + L_QX + ((2u * w + u) * 5u + m) * 1024u + lane * 16u, pack8(qa[u][0][m], qa[u][1][m]));
  barrier_lds();
  frag Q[8][5];
  {
    const unsigned qa0 = lb + L_QX + lane * 16u;
#define RDQ(s) lds_rd16_acc<(s * 5 + 0) * 1024>(Q[s][0], qa0); lds_rd16_acc<(s * 5 + 1) * 1024>(Q[s][1], qa0); lds_rd16_acc<(s * 5 + 2) * 1024>(Q[s][2], qa0); \
               lds_rd16_acc<(s * 5 + 3) * 1024>(Q[s][3], qa0); lds_rd16_acc<(s * 5 + 4) * 1024>(Q[s][4], qa0);
    RDQ(0) RDQ(1) RDQ(2) RDQ(3) RDQ(4) RDQ(5) RDQ(6) RDQ(7)
#undef RDQ
  }
#define QOPS(s) "+a"(Q[s][0]), "+a"(Q[s][1]), "+a"(Q[s][2]), "+a"(Q[s][3]), "+a"(Q[s][4])
  asm volatile("s_waitcnt lgkmcnt(0)\n\ts_barrier" : QOPS(0), QOPS(1), QOPS(2), QOPS(3) :: "memory");
  asm volatile("" : QOPS(4), QOPS(5), QOPS(6), QOPS(7) :: "memory");
#undef QOPS
#pragma unroll
  for (int c = 0; c < 4; ++c) dma4_v<4096>(vbase + c * 4096u, v_voff, sV, (32u * c) * 512u);

  const unsigned kr = kbase + (r16 >> 1) * 1056u + (r16 & 1u) * 512u + ((g ^ (r16 & 1u)) << 4);
  const unsigned fv = (2u * g + (q4 >> 1)) & 3u;
  unsigned vr[4];
#pragma unroll
  for (int c = 0; c < 4; ++c) vr[c] = vbase + (4u * g + q4) * 128u + ((((c ^ fv) << 1) | (p4 >> 1)) << 4) + (p4 & 1u) * 8u;
  unsigned pa = lb + L_P + lane * 16u, pw = pa + w * 5120u;
  unsigned ma = lb + L_MX + r16 * 4u;
  asm volatile("" : "+v"(pa), "+v"(pw), "+v"(ma), "+v"(vr[0]), "+v"(vr[1]), "+v"(vr[2]), "+v"(vr[3]));

  float mrow[5], lrow[5];
  frag P[5];
#pragma unroll
  for (int m = 0; m < 5; ++m) { mrow[m] = -__builtin_inff(); lrow[m] = 0.f; P[m] = frag{0u, 0u, 0u, 0u}; }

  frag ka[4], kb[4], pA[5], pB[5];
  u32x2 va[8], vb[8];
  auto rdK1 = [&](frag (&f)[4], int u, int half, int s) { f[s] = lds_rd16(kr + u * KSLOT + (4 * half + s) * 64u); };
  auto rdV1 = [&](u32x2 (&f)[8], int c, int i) { f[i] = lds_tr1(vr[i >> 1] + c * 4096u + (i & 1) * 2048u); };
  auto rdP1 = [&](frag (&f)[5], int c, int m) { f[m] = lds_rd16(pa + (c * 5 + m) * 1024u); };
  auto grpQK = [&](f32x4 (&S)[5], frag (&f)[4], auto half_c, auto fill) {
    constexpr int half = decltype(half_c)::value;
    for20([&](auto i_c) {
      constexpr int I = decltype(i_c)::value, s = I / 5, m = I % 5;
      if (half == 0 && s == 0) mm_va0(S[m], f[s], Q[s][m]); else mm_va(S[m], f[s], Q[4 * half + s][m]);
      fill(i_c); SBAR();
    });
  };
  auto grpPV = [&](u32x2 (&vf)[8], frag (&pf)[5], auto fill) {
    for20([&](auto i_c) {
      constexpr int I = decltype(i_c)::value, m = I / 4, d = I % 4;
      mm_av(O[d][m], __builtin_shufflevector(vf[2 * d], vf[2 * d + 1], 0, 1, 2, 3), pf[m]);
      fill(i_c); SBAR();
    });
  };
  auto kpiece = [&](int u, auto i_c, unsigned kso) {
    constexpr int I = decltype(i_c)::value;
    dma_kv<1024 * (I & 3)>(kbase + u * KSLOT + 32u * I + 4096u * (I >> 2), k_voff, sK, kso + u * 8192u + 4096u * (I >> 2));
  };
  auto vpiece = [&](int c, auto i_c, unsigned vso) {
    constexpr int I = decltype(i_c)::value;
    dma_kv<1024 * I>(vbase + c * 4096u, v_voff, sV, vso + c * 16384u + 3072u * I);
  };
  auto pv = [&](auto last_tag, unsigned vso, auto extra) {
    constexpr bool LAST = decltype(last_tag)::value;
    auto fill = [&](u32x2 (&vn)[8], frag (&pn)[5], auto c_c) {
      return [&vn, &pn, &rdV1, &rdP1, &vpiece, &extra, vso](auto i_c) {
        constexpr int I = decltype(i_c)::value, C = decltype(c_c)::value;
        if constexpr (C < 3 && I < 8) rdV1(vn, C + 1, I);
        if constexpr (C < 3 && I >= 8 && I < 13) rdP1(pn, C + 1, I - 8);
        if constexpr (!LAST && C > 0 && I % 5 == 3) vpiece(C - 1, IC<I / 5>{}, vso);
        if constexpr (LAST && I % 5 == 3) extra(IC<4 * C + I / 5>{});
      };
    };
#pragma unroll
    for (int m = 0; m < 5; ++m) rdP1(pA, 0, m);
    if (LAST) wait_vm<8>(); else wait_vm<24>();
    SBAR(); grpPV(va, pA, fill(vb, pB, IC<0>{}));
    if (LAST) wait_vm<8>(); else wait_vm<20>();
    grpPV(vb, pB, fill(va, pA, IC<1>{}));
    if (LAST) wait_vm<8>(); else wait_vm<20>();
    grpPV(va, pA, fill(vb, pB, IC<2>{}));
    grpPV(vb, pB, fill(va, pA, IC<3>{}));
  };

  wait_vm<24>();
#pragma unroll
  for (int s = 0; s < 4; ++s) rdK1(ka, 0, 0, s);
#pragma unroll 1
  for (unsigned j = 0; j < 8u; ++j) {
    f32x4 S0[5], S1[5];
    float mx[5];
    STAMP(3 + 2 * j);
    const unsigned kso = (128u * (j + 1u) + 32u * w) * 512u;
    SBAR();
    grpQK(S0, ka, IC<0>{}, [&](auto i_c) {
      constexpr int I = decltype(i_c)::value;
      if constexpr (I < 4) rdK1(kb, 0, 1, I);
      if constexpr (I >= 5 && I % 3 == 2) lds_wr16(pw + ((I - 5) / 3) * 1024u, P[(I - 5) / 3]);
    });
    wait_vm_eq<1, 0, 16>(j);
    grpQK(S0, kb, IC<1>{}, [&](auto i_c) {
      constexpr int I = decltype(i_c)::value;
      if constexpr (I < 4) rdK1(ka, 1, 0, I);
      if constexpr (I == 4) asm volatile("s_waitcnt lgkmcnt(4)" ::: "memory");
      if constexpr (I % 5 == 4) kpiece(0, IC<I / 5>{}, kso);
    });
    grpQK(S1, ka, IC<0>{}, [&](auto i_c) {
      constexpr int I = decltype(i_c)::value;
      if constexpr (I < 4) rdK1(kb, 1, 1, I);
      if constexpr (I % 5 == 4) kpiece(0, IC<I / 5 + 4>{}, kso);
      if constexpr (I >= 10 && I < 15) mx[I - 10] = max3f(S0[I - 10][0], S0[I - 10][1], S0[I - 10][2]);
      if constexpr (I >= 15) mx[I - 15] = max2f(mx[I - 15], S0[I - 15][3]);
    });
    asm volatile("s_waitcnt lgkmcnt(0)" ::: "memory");
    wait_vm<20>();
    grpQK(S1, kb, IC<1>{}, [&](auto i_c) {
      constexpr int I = decltype(i_c)::value;
      if constexpr (I < 8) rdV1(va, 0, I);
      if constexpr (I % 5 == 4) kpiece(1, IC<I / 5>{}, kso);
    });
    kpiece(1, IC<4>{}, kso); kpiece(1, IC<5>{}, kso);
    asm volatile("s_nop 1" ::: "memory");
    SBAR();
#pragma unroll
    for (int m = 0; m < 5; ++m) mx[m] = max3f(mx[m], S1[m][0], S1[m][1]);
#pragma unroll
    for (int m = 0; m < 5; ++m) mx[m] = max3f(mx[m], S1[m][2], S1[m][3]);
    SBAR();
    lds_max<0>(ma, mx[0]); lds_max<64>(ma, mx[1]); lds_max<128>(ma, mx[2]); lds_max<192>(ma, mx[3]); lds_max<256>(ma, mx[4]);
    kpiece(1, IC<6>{}, kso); kpiece(1, IC<7>{}, kso);
    barrier_lds();
    STAMP(4 + 2 * j);
    float cm[5];
#pragma unroll
    for (int m = 0; m < 5; ++m) cm[m] = lds_rdf(ma + m * 64u);
    if (j > 0u) pv(FF{}, j * 65536u, [](auto) {});
    float al[5];
    float gr = max3f(cm[0] - mrow[0], cm[1] - mrow[1], cm[2] - mrow[2]);
    gr = max3f(gr, cm[3] - mrow[3], cm[4] - mrow[4]);
#pragma unroll
    for (int m = 0; m < 5; ++m) al[m] = 1.f;
    if (__builtin_expect(__any(gr > RESCALE_THR / SM_C), 0)) {
#pragma unroll
      for (int m = 0; m < 5; ++m) { al[m] = __builtin_amdgcn_exp2f((mrow[m] - cm[m]) * SM_C); mrow[m] = cm[m]; }
      if (j > 0u) {
        asm volatile("s_nop 7" ::: "memory");
#pragma unroll
        for (int d = 0; d < 4; ++d)
#pragma unroll
          for (int m = 0; m < 5; ++m) { asm volatile("" : "+a"(O[d][m])); O[d][m] *= al[m]; asm volatile("" : "+a"(O[d][m])); }
      }
    }
    {
      const unsigned vso = j * 65536u;
      float nm[5];
#pragma unroll
      for (int m = 0; m < 5; ++m) nm[m] = -mrow[m] * SM_C;
#pragma unroll
      for (int r = 0; r < 4; ++r)
#pragma unroll
        for (int m = 0; m < 5; ++m) { S0[m][r] = fmaf(S0[m][r], SM_C, nm[m]); S1[m][r] = fmaf(S1[m][r], SM_C, nm[m]); }
      SBAR(); dma_kv_if<0>(j, vbase + 12288u, v_voff, sV, vso + 49152u); SBAR();
#pragma unroll
      for (int m = 0; m < 5; ++m) { S0[m][0] = __builtin_amdgcn_exp2f(S0[m][0]); S0[m][1] = __builtin_amdgcn_exp2f(S0[m][1]); }
      SBAR(); dma_kv_if<1024>(j, vbase + 12288u, v_voff, sV, vso + 49152u + 3072u); SBAR();
#pragma unroll
      for (int m = 0; m < 5; ++m) { S0[m][2] = __builtin_amdgcn_exp2f(S0[m][2]); S0[m][3] = __builtin_amdgcn_exp2f(S0[m][3]); }
      SBAR(); dma_kv_if<2048>(j, vbase + 12288u, v_voff, sV, vso + 49152u + 6144u); SBAR();
#pragma unroll
      for (int m = 0; m < 5; ++m) { S1[m][0] = __builtin_amdgcn_exp2f(S1[m][0]); S1[m][1] = __builtin_amdgcn_exp2f(S1[m][1]); }
      SBAR(); dma_kv_if<3072>(j, vbase + 12288u, v_voff, sV, vso + 49152u + 9216u); SBAR();
#pragma unroll
      for (int m = 0; m < 5; ++m) { S1[m][2] = __builtin_amdgcn_exp2f(S1[m][2]); S1[m][3] = __builtin_amdgcn_exp2f(S1[m][3]); }
      SBAR();
      wait_vm_eq<0, 8, 24>(j);
#pragma unroll
      for (int s = 0; s < 4; ++s) rdK1(ka, 0, 0, s);
      SBAR();
      float t0[5], t1[5];
#pragma unroll
      for (int m = 0; m < 5; ++m) { t0[m] = addf(S0[m][0], S0[m][1]); t1[m] = addf(S0[m][2], S0[m][3]); }
#pragma unroll
      for (int m = 0; m < 5; ++m) { t0[m] = addf(t0[m], S1[m][0]); t1[m] = addf(t1[m], S1[m][1]); }
#pragma unroll
      for (int m = 0; m < 5; ++m) { t0[m] = addf(t0[m], S1[m][2]); t1[m] = addf(t1[m], S1[m][3]); }
#pragma unroll
      for (int m = 0; m < 5; ++m) P[m] = pack8(S0[m], S1[m]);
#pragma unroll
      for (int m = 0; m < 5; ++m) lrow[m] = fmaf(lrow[m], al[m], addf(t0[m], t1[m]));
      SBAR();
    }
    asm volatile("s_barrier" ::: "memory");
  }
  STAMP(19);
  const unsigned sa = lb + L_ST + r16 * 16u;
#pragma unroll
  for (int m = 0; m < 5; ++m) lds_wr16(pw + m * 1024u, P[m]);
#pragma unroll
  for (int m = 0; m < 5; ++m) lds_wrf(sa + m * 256u + w * 4u, xsum_g(lrow[m]));
  const srd_t sO = mk_srd((br ? woB : woA) + (size_t)h * (DH * DM * 2) + w * 224u, DH * DM * 2 - w * 224u);
  unsigned wo_voff[7];
#pragma unroll
  for (int i = 0; i < 7; ++i) { const unsigned n = i * 64u + lane, row = (n * 4682u) >> 16; wo_voff[i] = row * 896u + (n - row * 14u) * 16u; }
  f32x4 acc[7][5];
#pragma unroll
  for (int n = 0; n < 7; ++n)
#pragma unroll
    for (int m = 0; m < 5; ++m) { acc[n][m] = f32x4{0.f, 0.f, 0.f, 0.f}; asm volatile("" : "+v"(acc[n][m])); }
  auto wopiece = [&](unsigned slot, unsigned so, auto i_c) { constexpr int I = decltype(i_c)::value; dma_o(slot + I * 1024u, wo_voff[I], sO, so); };
  wait_vm<12>();
#pragma unroll
  for (int i = 0; i < 8; ++i) rdV1(va, 0, i);
  barrier_lds();
  STAMP(20);
  f32x4 st[5];
#pragma unroll
  for (int m = 0; m < 5; ++m) st[m] = lds_rdf4(sa + m * 256u);
  pv(TT{}, 0u, [&](auto i_c) {
    constexpr int I = decltype(i_c)::value;
    if constexpr (I < 7) wopiece(kbase, b * 28672u, i_c);
    else if constexpr (I < 14) wopiece(kbase + 7168u, ((b + 1u) & 7u) * 28672u, IC<I - 7>{});
  });
  float inv[5];
#pragma unroll
  for (int m = 0; m < 5; ++m) inv[m] = 1.0f / ((st[m][0] + st[m][1]) + (st[m][2] + st[m][3]));
  asm volatile("s_nop 7" ::: "memory");
  STAMP(21);
#pragma unroll
  for (int m = 0; m < 5; ++m)
#pragma unroll
    for (int u = 0; u < 2; ++u) {
      asm volatile("" : "+a"(O[2 * u][m]), "+a"(O[2 * u + 1][m]));
      lds_wr16(vbase + (u * 5 + m) * 1024u + lane * 16u, pack8(O[2 * u][m] * inv[m], O[2 * u + 1][m] * inv[m]));
    }
  barrier_lds();
  STAMP(22);
  {
    unsigned s0 = kbase, s1 = kbase + 7168u, s2 = lb + L_P + w * 7168u;
    for_seq([&](auto i_c) { wopiece(s2, ((b + 2u) & 7u) * 28672u, i_c); }, std::make_integer_sequence<int, 7>{});
    const unsigned ar = (4u * g + q4) * 224u + p4 * 8u, ob = lb + L_V + lane * 16u;
    auto oaddr = [&](unsigned s) { const unsigned k = (s + b) & 7u; return ob + (k >> 1) * 16384u + (k & 1u) * 5120u; };
    u32x2 wf[14];
    frag oa[5], obb[5];
    wait_vm<14>();
#pragma unroll
    for (int i = 0; i < 14; ++i) wf[i] = lds_tr1(s0 + ar + (i >> 1) * 32u + (i & 1) * 3584u);
#pragma unroll
    for (int m = 0; m < 5; ++m) oa[m] = lds_rd16(oaddr(0u) + m * 1024u);
    auto step = [&](frag (&oc)[5], frag (&on)[5], unsigned s) {
      const unsigned an = s1 + ar, oan = oaddr(s + 1u), so = s < 5u ? ((s + 3u + b) & 7u) * 28672u : 0x40000000u, slot = s0;
      wait_vm<7>();
      SBAR();
      for_seq([&](auto i_c) {
        constexpr int I = decltype(i_c)::value, n = I / 5, m = I % 5;
        mm_vv(acc[n][m], __builtin_shufflevector(wf[2 * n], wf[2 * n + 1], 0, 1, 2, 3), oc[m]);
        if constexpr (I < 4) on[I] = lds_rd16(oan + I * 1024u);
        if constexpr (I == 6) on[4] = lds_rd16(oan + 4096u);
        if constexpr (m == 4) wf[2 * n] = lds_tr1(an + n * 32u);
        if constexpr ((m == 0 && n > 0) || I == 34) wf[I == 34 ? 13 : 2 * n - 1] = lds_tr1(an + (I == 34 ? 6 : n - 1) * 32u + 3584u);
        if constexpr (I == 7) asm volatile("s_waitcnt lgkmcnt(7)" ::: "memory");
        if constexpr (I >= 7 && I % 4 == 3) wopiece(slot, so, IC<(I - 7) / 4>{});
        SBAR();
      }, std::make_integer_sequence<int, 35>{});
      const unsigned t = s0; s0 = s1; s1 = s2; s2 = t;
    };
#pragma unroll 1
    for (unsigned s = 0; s < 8u; s += 2u) { step(oa, obb, s); step(obb, oa, s + 1u); }
  }
  asm volatile("s_nop 7" ::: "memory");
  STAMP(24);
  const srd_t sP = mk_srd(ws + (size_t)grp * (HQ * PART_B), HQ * PART_B);
  unsigned long long ackv;
  asm volatile("global_load_dwordx2 %0, %1, %2 sc1" : "=v"(ackv) : "v"(r16 * 128u + h * 8u), "s"(ackp) : "memory");
  {
    const unsigned po = h * PART_B + lane * 16u;
#pragma unroll
    for (int i = 0; i < 17; ++i) {
      asm volatile("" : "+v"(acc[(2 * i) / 5][(2 * i) % 5]), "+v"(acc[(2 * i + 1) / 5][(2 * i + 1) % 5]));
      __builtin_amdgcn_raw_buffer_store_b128(pack8(acc[(2 * i) / 5][(2 * i) % 5], acc[(2 * i + 1) / 5][(2 * i + 1) % 5]), sP, po + i * 1024u, w * 17408u, PART_AUX);
    }
    asm volatile("" : "+v"(acc[6][4]));
    __builtin_amdgcn_raw_buffer_store_b64(u32x2{cvtpk(acc[6][4][0], acc[6][4][1]), cvtpk(acc[6][4][2], acc[6][4][3])}, sP, po + (w & 1u) * 8u, (68u + (w >> 1)) * 1024u, PART_AUX);
  }
  unsigned lo[5], oo[5];
#pragma unroll
  for (int t = 0; t < 5; ++t) {
    const unsigned c = 16u * t + r16, gm = (h * 4u + w) * 70u + c, U = gm >> 6, l2 = gm & 63u;
    lo[t] = c < 70u ? g * PART_B + gm * 16u : 0x40000000u;
    const unsigned wv = U < 68u ? (U * 3856u) >> 16 : 2u * (U - 68u) + (g & 1u);
    const unsigned tl = U < 68u ? 2u * (U - 17u * wv) + (g & 1u) : 34u, n = (tl * 52u) >> 8, m = tl - 5u * n;
    oo[t] = c < 70u && g < 2u ? (16u * m + (l2 & 15u)) * (2 * DM * 2) + (112u * wv + 16u * n + 4u * (l2 >> 4)) * 2u : 0x40000000u;
  }
  asm volatile("s_waitcnt vmcnt(0)" : "+v"(ackv) :: "memory");
  asm volatile("s_barrier" ::: "memory");
  STAMP(25);
  if (w == 0u) {
    const unsigned long long mine = ((unsigned long long)(unsigned)__builtin_amdgcn_readlane((unsigned)(tag >> 32), h) << 32) | (unsigned)__builtin_amdgcn_readlane((unsigned)tag, h);
    for (unsigned spin = 0; !__all(ackv == mine) && spin < (1u << 18); ++spin) {
      __builtin_amdgcn_s_sleep(1);
      ackv = __hip_atomic_load((gu64*)(ackp + r16 * 128u + h * 8u), RLX_AGENT);
    }
    if (lane == 0u) __hip_atomic_store((gu64*)(flagp + h * 8u), mine, RLX_AGENT);
    for (unsigned spin = 0; spin < (1u << 18); ++spin) {
      if (__all(__hip_atomic_load((gu64*)(flagp + r16 * 8u), RLX_AGENT) == tag)) break;
      __builtin_amdgcn_s_sleep(1);
    }
  }
  asm volatile("s_barrier" ::: "memory");
  STAMP(26);
  {
    frag pf[5][4];
#pragma unroll
    for (int t = 0; t < 5; ++t)
#pragma unroll
      for (int k = 0; k < 4; ++k) pf[t][k] = __builtin_amdgcn_raw_buffer_load_b128(sP, lo[t], k * 4u * PART_B, PART_AUX);
    frag sel;
#pragma unroll
    for (int d = 0; d < 4; ++d) sel[d] = (r16 >> 1) == (unsigned)d ? ((r16 & 1u) ? 0x3F800000u : 0x00003F80u) : 0u;
    const srd_t sOut = mk_srd(out + (size_t)b * (SQ * 2 * DM * 2) + br * (DM * 2), SQ * 2 * DM * 2 - br * (DM * 2));
#pragma unroll
    for (int t = 0; t < 5; ++t) {
      f32x4 r = {0.f, 0.f, 0.f, 0.f};
#pragma unroll
      for (int k = 0; k < 4; ++k) r = MFMA16(__builtin_bit_cast(bf16x8, sel), __builtin_bit_cast(bf16x8, pf[t][k]), r);
      __builtin_amdgcn_raw_buffer_store_b64(u32x2{cvtpk(r[0], r[1]), cvtpk(r[2], r[3])}, sOut, oo[t], 0, 0);
    }
  }
  STAMP(27);
  STAMP_FLUSH();
  asm volatile(".Lgqa_end:");
}

extern "C" void kernel_launch(void* const* d_in, const int* in_sizes, int n_in, void* d_out, int out_size, void* d_ws, size_t ws_size,
                              hipStream_t stream) {
  gqa_block_fused<<<256, 256, 0, stream>>>((const char*)d_in[0], (const char*)d_in[1], (const char*)d_in[2], (const char*)d_in[3], (const char*)d_in[4],
                                           (const char*)d_in[5], (const char*)d_in[6], (const char*)d_in[7], (const char*)d_in[8], (const char*)d_in[9],
                                           (char*)d_ws, (char*)d_out);
}
```

```cpp
#include <hip/hip_runtime.h>
#include <hip/hip_bf16.h>
#include <utility>

typedef float f32x4 __attribute__((ext_vector_type(4)));
typedef float f32x2 __attribute__((ext_vector_type(2)));
typedef __bf16 bf16x8 __attribute__((ext_vector_type(8)));
typedef __bf16 bf16x4 __attribute__((ext_vector_type(4)));
typedef __bf16 bf16x2 __attribute__((ext_vector_type(2)));
typedef unsigned u32x4 __attribute__((ext_vector_type(4)));
typedef unsigned u32x2 __attribute__((ext_vector_type(2)));
typedef u32x4 frag;
typedef __amdgpu_buffer_rsrc_t srd_t;
typedef __attribute__((address_space(1))) unsigned long long gu64;
#define DI __device__ __forceinline__
#define LDS3 __attribute__((address_space(3)))
#define SBAR() __builtin_amdgcn_sched_barrier(0)
#define RLX_AGENT __ATOMIC_RELAXED, __HIP_MEMORY_SCOPE_AGENT

#define KV_POL " nt"
#define W_POL ""
#define O_POL ""
#define RESCALE_THR 8.0f
#define PART_AUX 16

#define STAMP(i) do {} while (0)
#define STAMP_FLUSH() do {} while (0)

constexpr int NB = 8, SQ = 80, DM = 448, HQ = 16, HKV = 8, SK = 1024, DH = 256;
constexpr float SM_C = 0.0625f * 1.4426950408889634f;

constexpr unsigned L_K = 0;
constexpr unsigned L_V = 65536;
constexpr unsigned L_P = 131072;
constexpr unsigned L_ST = 151552;
constexpr unsigned L_MX = L_ST + 1280;
constexpr unsigned L_X = L_V;
constexpr unsigned L_QX = 98304;
constexpr unsigned LDS1 = 163840;

constexpr unsigned PART_B = 71680;
constexpr unsigned WS_FLAG = 24u << 20, WS_ACK = WS_FLAG + 65536u;
constexpr unsigned long long HO_STEP = 0x9E3779B97F4A7C15ull;

template <int I> struct IC { static constexpr int value = I; };
template <class F, int... Is> DI void for_seq(F&& f, std::integer_sequence<int, Is...>) { (f(IC<Is>{}), ...); }
template <int N, class F> DI void forN(F&& f) { for_seq(f, std::make_integer_sequence<int, N>{}); }

DI unsigned cvtpk(float lo, float hi) { f32x2 v = {lo, hi}; bf16x2 b = __builtin_convertvector(v, bf16x2); return __builtin_bit_cast(unsigned, b); }
DI unsigned cvtpk_here(float lo, float hi) { unsigned r = cvtpk(lo, hi); asm volatile("" : "+v"(r)); return r; }
DI frag pack8(const f32x4& a, const f32x4& b) { return u32x4{cvtpk(a[0], a[1]), cvtpk(a[2], a[3]), cvtpk(b[0], b[1]), cvtpk(b[2], b[3])}; }
DI frag cat2(const u32x2& lo, const u32x2& hi) { return __builtin_shufflevector(lo, hi, 0, 1, 2, 3); }
DI frag lds_rd16(unsigned a) { return *(const LDS3 frag*)(size_t)a; }
DI f32x4 lds_rdf4(unsigned a) { return *(const LDS3 f32x4*)(size_t)a; }
DI float lds_rdf(unsigned a) { return *(const LDS3 float*)(size_t)a; }
DI void lds_wr16(unsigned a, frag v) { *(LDS3 frag*)(size_t)a = v; }
DI void lds_wrf(unsigned a, float v) { *(LDS3 float*)(size_t)a = v; }
DI u32x2 lds_tr1(unsigned a) { return __builtin_bit_cast(u32x2, __builtin_amdgcn_ds_read_tr16_b64_v4bf16((LDS3 bf16x4*)(size_t)a)); }
template <int OFF> DI void lds_max(unsigned a, float v) { asm volatile("ds_max_f32 %0, %1 offset:%2" ::"v"(a), "v"(v), "i"(OFF) : "memory"); }
template <int OFF> DI void lds_rd16_acc(frag& d, unsigned a) { asm volatile("ds_read_b128 %0, %1 offset:%2" : "=a"(d) : "v"(a), "i"(OFF) : "memory"); }
DI srd_t mk_srd(const void* p, unsigned bytes) { return __builtin_amdgcn_make_buffer_rsrc((void*)p, 0, bytes, 0x00020000); }
template <int N> DI void wait_vm() { asm volatile("s_waitcnt vmcnt(%0)" ::"i"(N) : "memory"); }
template <int N> DI void wait_lgkm() { asm volatile("s_waitcnt lgkmcnt(%0)" ::"i"(N) : "memory"); }
DI void barrier_lds() { asm volatile("s_waitcnt lgkmcnt(0)\n\ts_barrier" ::: "memory"); }
DI void barrier_raw() { asm volatile("s_barrier" ::: "memory"); }
DI float xsum_g(float x) {
  auto a = __builtin_amdgcn_permlane16_swap(__float_as_uint(x), __float_as_uint(x), false, false);
  x = __uint_as_float(a[0]) + __uint_as_float(a[1]);
  auto b = __builtin_amdgcn_permlane32_swap(__float_as_uint(x), __float_as_uint(x), false, false);
  return __uint_as_float(b[0]) + __uint_as_float(b[1]);
}
DI float max3f(float a, float b, float c) { float r; asm("v_max3_f32 %0, %1, %2, %3" : "=v"(r) : "v"(a), "v"(b), "v"(c)); return r; }
DI float max2f(float a, float b) { float r; asm("v_max_f32_e32 %0, %1, %2" : "=v"(r) : "v"(a), "v"(b)); return r; }
DI float addf(float a, float b) { float r; asm("v_add_f32_e32 %0, %1, %2" : "=v"(r) : "v"(a), "v"(b)); return r; }

DI void set_m0(unsigned lds) { asm volatile("s_mov_b32 m0, %0" ::"s"(lds) : "memory"); }
DI void set_m0_now(unsigned lds) { asm volatile("s_mov_b32 m0, %0\n\ts_nop 0" ::"s"(lds) : "memory"); }
#define DMA_FN(NAME, POL)                                                                                                              \
  template <int IMM> DI void NAME(unsigned voff, srd_t srd, unsigned soff) {                                                           \
    asm volatile("buffer_load_dwordx4 %0, %1, %2 offen offset:%3" POL " lds" ::"v"(voff), "s"(srd), "s"(soff), "i"(IMM) : "memory");   \
  }                                                                                                                                    \
  template <int IMM, int K> DI void NAME##_lt(unsigned c, unsigned voff, srd_t srd, unsigned soff) {              \
    asm volatile("s_cmp_ge_u32 %4, %5\n\ts_cbranch_scc1 1f\n\tbuffer_load_dwordx4 %0, %1, %2 offen offset:%3" POL " lds\n1:"           \
                 ::"v"(voff), "s"(srd), "s"(soff), "i"(IMM), "s"(c), "i"(K) : "memory", "scc");                                        \
  }
DMA_FN(dma_kv, KV_POL)
DMA_FN(dma_w, W_POL)
DMA_FN(dma_o, O_POL)
DI void ld_kv(frag& d, unsigned voff, srd_t srd, unsigned soff) {
  asm volatile("buffer_load_dwordx4 %0, %1, %2, %3 offen" KV_POL : "=v"(d) : "v"(voff), "s"(srd), "s"(soff) : "memory");
}
template <int K, int NA, int NB> DI void wait_vm_lt(unsigned v) {
  if constexpr (NA < NB) asm volatile("s_cmp_ge_u32 %0, %3\n\ts_cbranch_scc1 1f\n\ts_waitcnt vmcnt(%1)\n1:\n\ts_waitcnt vmcnt(%2)" ::"s"(v), "i"(NA), "i"(NB), "i"(K) : "memory", "scc");
  else asm volatile("s_cmp_lt_u32 %0, %3\n\ts_cbranch_scc1 1f\n\ts_waitcnt vmcnt(%2)\n1:\n\ts_waitcnt vmcnt(%1)" ::"s"(v), "i"(NA), "i"(NB), "i"(K) : "memory", "scc");
}
template <int K1, int N1, int K2, int N2, int K3, int N3> DI void wait_vm_3(unsigned v) {
  asm volatile("s_cmp_ge_u32 %0, %5\n\ts_cbranch_scc1 3f\n\ts_cmp_lt_u32 %0, %1\n\ts_cbranch_scc1 1f\n\ts_cmp_lt_u32 %0, %3\n\ts_cbranch_scc1 2f\n\t"
               "s_waitcnt vmcnt(%6)\n2:\n\ts_waitcnt vmcnt(%4)\n1:\n\ts_waitcnt vmcnt(%2)\n3:"
               ::"s"(v), "i"(K1), "i"(N1), "i"(K2), "i"(N2), "i"(K3), "i"(N3) : "memory", "scc");
}
template <int K0, int N0, int K1, int N1, int K2, int N2, int K3, int N3> DI void wait_vm_4(unsigned v) {
  asm volatile("s_cmp_lt_u32 %0, %1\n\ts_cbranch_scc1 0f\n\ts_cmp_lt_u32 %0, %3\n\ts_cbranch_scc1 1f\n\ts_cmp_lt_u32 %0, %5\n\ts_cbranch_scc1 2f\n\t"
               "s_waitcnt vmcnt(%7)\n2:\n\ts_waitcnt vmcnt(%6)\n1:\n\ts_waitcnt vmcnt(%4)\n0:\n\ts_waitcnt vmcnt(%2)"
               ::"s"(v), "i"(K0), "i"(N0), "i"(K1), "i"(N1), "i"(K2), "i"(N2), "i"(N3) : "memory", "scc");
}
DI void mm_va0(f32x4& d, const frag& a, const frag& bq) { asm volatile("v_mfma_f32_16x16x32_bf16 %0, %1, %2, 0" : "=&v"(d) : "v"(a), "a"(bq)); }
DI void mm_va(f32x4& d, const frag& a, const frag& bq) { asm volatile("v_mfma_f32_16x16x32_bf16 %0, %1, %2, %0" : "+v"(d) : "v"(a), "a"(bq)); }
DI void mm_av(f32x4& d, const frag& a, const frag& b) { asm volatile("v_mfma_f32_16x16x32_bf16 %0, %1, %2, %0" : "+a"(d) : "v"(a), "v"(b)); }
DI void mm_vv(f32x4& d, const frag& a, const frag& b) { asm volatile("v_mfma_f32_16x16x32_bf16 %0, %1, %2, %0" : "+v"(d) : "v"(a), "v"(b)); }
#define PIN_V(x) asm volatile("" : "+v"(x))
#define PIN_A(x) asm volatile("" : "+a"(x))
#define PIN_S(x) asm volatile("" : "+s"(x))

__global__ __launch_bounds__(256, 1) void gqa_block_fused(const char* __restrict__ xA, const char* __restrict__ kA, const char* __restrict__ vA,
                                                          const char* __restrict__ wqA, const char* __restrict__ woA, const char* __restrict__ xB,
                                                          const char* __restrict__ kB, const char* __restrict__ vB, const char* __restrict__ wqB,
                                                          const char* __restrict__ woB, char* ws, char* __restrict__ out) {
  __shared__ __attribute__((aligned(16))) char lds_mem[LDS1];
  const unsigned lb = (unsigned)(size_t)(LDS3 char*)lds_mem;
  const unsigned tid = threadIdx.x, lane = tid & 63u;
  const unsigned w = __builtin_amdgcn_readfirstlane(tid >> 6);
  const unsigned bid = blockIdx.x;
  asm volatile("" ::"s"(xA), "s"(kA), "s"(vA), "s"(wqA), "s"(woA), "s"(xB), "s"(kB), "s"(vB), "s"(wqB), "s"(woB), "s"(ws), "s"(out));
  const unsigned idx = bid >> 3, xg = bid & 7u;
  const unsigned combo = 2u * xg + (idx >> 4), br = combo >> 3, kvh = combo & 7u, b = (idx & 15u) >> 1, h = 2u * kvh + (idx & 1u);
  const unsigned r16 = lane & 15u, g = lane >> 4, q4 = r16 >> 2, p4 = lane & 3u;
  const char* const pK = (br ? kB : kA) + (size_t)(b * HKV + kvh) * (SK * DH * 2);
  const char* const pO = (br ? woB : woA) + (size_t)h * (DH * DM * 2);
  const srd_t sX = mk_srd((br ? xB : xA) + (size_t)b * (SQ * DM * 2), SQ * DM * 2);
  const srd_t sW = mk_srd((br ? wqB : wqA) + h * (DH * 2), DM * HQ * DH * 2 - h * (DH * 2));
  const srd_t sK = mk_srd(pK, SK * DH * 2);
  const srd_t sV = mk_srd((br ? vB : vA) + (size_t)(b * HKV + kvh) * (SK * DH * 2), SK * DH * 2);
  const srd_t sO = mk_srd(pO, DH * DM * 2);
  STAMP(0);
  const unsigned kbase = lb + L_K + w * 16384u, vbase = lb + L_V + w * 8192u;
  unsigned p0 = (b + 2u * (h & 3u)) % 7u;
  unsigned p1 = p0 == 6u ? 0u : p0 + 1u;
  unsigned xv[3], fa[4];
  {
#pragma unroll
    for (unsigned t = 0; t < 3u; ++t) {
      const unsigned pc = 3u * w + t;
      xv[t] = pc < 10u ? (pc * 8u + (lane >> 3)) * 896u + (((lane & 7u) ^ ((4u * (pc & 1u) + (lane >> 4)) & 7u)) << 4) - t * 1024u : 0x40000000u;
    }
    const unsigned wq_voff = (lane >> 3) * 8192u + (((lane & 7u) ^ (((lane >> 4) & 1u) << 2)) << 4) + w * 128u;
#pragma unroll
    for (int i = 0; i < 4; ++i) fa[i] = wq_voff + i * 64512u;
  }
  const unsigned xm = lb + L_X + w * 3072u;
  auto xblock = [&](unsigned pp, unsigned pb) {
    set_m0_now(xm + pp * 12288u);
    dma_w<0>(xv[0], sX, pb * 128u); dma_w<1024>(xv[1], sX, pb * 128u); dma_w<2048>(xv[2], sX, pb * 128u);
  };
  xblock(0u, p0);
#pragma unroll
  for (unsigned e = 0; e < 2u; ++e) {
    set_m0_now(kbase + 8192u + e * 4096u);
    forN<4>([&](auto i_c) { constexpr int I = decltype(i_c)::value; dma_w<1024 * I>(fa[I], sW, p0 * 524288u + e * 262144u); });
  }
  const unsigned grp = br * NB + b;
  char* const flagp = ws + WS_FLAG + grp * 128u;
  char* const ackp = ws + WS_ACK + grp * 2048u;
  char* const partp = ws + (size_t)grp * (HQ * PART_B);
  char* const outp = out + (size_t)b * (SQ * 2 * DM * 2) + br * (DM * 2);
  unsigned long long tag;
  asm volatile("global_load_dwordx2 %0, %1, %2 sc1" : "=v"(tag) : "v"(r16 * 8u), "s"(flagp) : "memory");
  xblock(1u, p1);
  const unsigned v_voff = (lane >> 3) * 512u + (((lane & 7u) ^ (((lane >> 4) & 3u) << 1)) << 4) + w * 128u;
  {
    const char* tp = lane < 18u ? partp + lane * 65536u : (lane < 22u ? pO + (lane - 18u) * 65536u : (lane < 24u ? outp + (lane - 22u) * 65536u : pK + ((lane - 24u) & 7u) * 65536u));
    set_m0_now(lb + L_MX + 512u);
    asm volatile("global_load_lds_dword %0, off" ::"v"(tp) : "memory");
  }
  unsigned kv[2][4];
#pragma unroll
  for (unsigned e = 0; e < 2u; ++e)
#pragma unroll
    for (unsigned i = 0; i < 4u; ++i) kv[e][i] = (lane >> 5) * 512u + (((lane & 31u) ^ (8u * e + 2u * i + (lane >> 5))) << 4);

  f32x4 qa[2][2][5];
#pragma unroll
  for (int u = 0; u < 2; ++u)
#pragma unroll
    for (int t = 0; t < 2; ++t)
#pragma unroll
      for (int m = 0; m < 5; ++m) { qa[u][t][m] = f32x4{0.f, 0.f, 0.f, 0.f}; PIN_V(qa[u][t][m]); }
  f32x4 O[4][5];
#pragma unroll
  for (int c = 0; c < 4; ++c)
#pragma unroll
    for (int m = 0; m < 5; ++m) { O[c][m] = f32x4{0.f, 0.f, 0.f, 0.f}; PIN_A(O[c][m]); }
  {
    unsigned xr0 = lb + L_X + r16 * 128u + ((g ^ (r16 >> 1)) << 4), xr1 = xr0 ^ 64u, xr0h = xr0 + 49152u, xr1h = xr1 + 49152u;
    PIN_V(xr0); PIN_V(xr1); PIN_V(xr0h); PIN_V(xr1h);
    unsigned wr0 = kbase + (8u * g + q4) * 128u + ((4u * (0u ^ (q4 >> 1)) + p4) << 4);
    unsigned wr1 = kbase + (8u * g + q4) * 128u + ((4u * (1u ^ (q4 >> 1)) + p4) << 4);
    PIN_V(wr0); PIN_V(wr1);
    frag xa[5], xb[5];
    u32x2 wa[8], wb[8];
    auto rd1 = [&](frag (&xf)[5], u32x2 (&wf)[8], unsigned xs, unsigned a0, unsigned a1, auto i_c) {
      constexpr int I = decltype(i_c)::value;
      if constexpr (I < 8) wf[I] = lds_tr1(((I >> 2) ? a1 : a0) + ((I >> 1) & 1) * 8u + (I & 1) * 512u);
      else if constexpr (I < 13) xf[I - 8] = lds_rd16(xs + (I - 8) * 2048u);
    };
    auto step = [&](frag (&xf)[5], u32x2 (&wf)[8], auto fill) {
      forN<20>([&](auto i_c) {
        constexpr int I = decltype(i_c)::value, m = I / 4, ut = I % 4;
        mm_vv(qa[ut >> 1][ut & 1][m], cat2(wf[2 * ut], wf[2 * ut + 1]), xf[m]);
        fill(i_c); SBAR();
      });
    };
    if (tid < 80u) lds_wrf(lb + L_MX + tid * 4u, -__builtin_inff());
    STAMP(23);
    wait_vm<9>();
    barrier_lds();
    STAMP(1);
    forN<13>([&](auto i_c) { rd1(xa, wa, xr0, wr0 + 8192u, wr1 + 8192u, i_c); });
    forN<7>([&](auto pp_c) {
      constexpr int PP = decltype(pp_c)::value;
      constexpr unsigned cur = (PP & 1) ? 0u : 8192u, nxt = 8192u - cur;
      const unsigned p2 = p1 == 6u ? 0u : p1 + 1u;
      unsigned so = PP < 5 ? p2 * 524288u : w * 16384u + (PP - 5) * 8192u, so2 = so + (PP < 5 ? 262144u : 4096u), xso = p2 * 128u, so1 = p1 * 524288u;
      PIN_S(so); PIN_S(so2); PIN_S(xso); PIN_S(so1);
      wait_vm<PP == 0 ? 5 : (PP < 6 ? 11 : 8)>();
      SBAR();
      step(xa, wa, [&](auto i_c) {
        constexpr int I = decltype(i_c)::value;
        rd1(xb, wb, (PP & 4 ? xr1h : xr1) + (PP & 3) * 12288u, wr0 + cur + 4096u, wr1 + cur + 4096u, i_c);
        if constexpr (PP == 0) {
          if constexpr (I == 0) set_m0(kbase);
          if constexpr (I >= 1 && I < 5) dma_w<1024 * (I - 1)>(fa[I - 1], sW, so1);
          if constexpr (I == 5) set_m0(kbase + 4096u);
          if constexpr (I >= 6 && I < 10) dma_w<1024 * (I - 6)>(fa[I - 6], sW, so1 + 262144u);
          if constexpr (I == 10) set_m0(xm + 2u * 12288u);
          if constexpr (I >= 11 && I < 14) dma_w<1024 * (I - 11)>(xv[I - 11], sX, xso);
          if constexpr (I == 14) set_m0(kbase + cur);
          if constexpr (I >= 15 && I < 19) dma_w<1024 * (I - 15)>(fa[I - 15], sW, so);
        } else if constexpr (PP < 5) {
          if constexpr (I == 0) set_m0(xm + (PP + 2) * 12288u);
          if constexpr (I == 1 || I == 4 || I == 7) dma_w<1024 * (I / 3)>(xv[I / 3], sX, xso);
          if constexpr (I == 9) set_m0(kbase + cur);
          if constexpr (I == 10 || I == 13 || I == 16 || I == 19) dma_w<1024 * ((I - 10) / 3)>(fa[(I - 10) / 3], sW, so);
        } else {
          if constexpr (I == 3) set_m0(kbase + cur);
          if constexpr (I % 5 == 4) dma_kv<1024 * (I / 5)>(kv[0][I / 5], sK, so);
        }
      });
      if constexpr (PP < 6) {
        wait_vm<PP < 5 ? 11 : 8>();
        barrier_raw();
      }
      if constexpr (PP == 0) {
        PIN_V(tag);
        tag += HO_STEP;
        if (tid < 16u) asm volatile("global_store_dwordx2 %0, %1, %2 sc1" ::"v"(h * 128u + tid * 8u), "v"(tag), "s"(ackp) : "memory");
      }
      SBAR();
      step(xb, wb, [&](auto i_c) {
        constexpr int I = decltype(i_c)::value;
        if constexpr (PP < 6) rd1(xa, wa, ((PP + 1) & 4 ? xr0h : xr0) + ((PP + 1) & 3) * 12288u, wr0 + nxt, wr1 + nxt, i_c);
        if constexpr (I == 3) set_m0(kbase + cur + 4096u);
        if constexpr (I % 5 == 4) { if constexpr (PP < 5) dma_w<1024 * (I / 5)>(fa[I / 5], sW, so2); else dma_kv<1024 * (I / 5)>(kv[1][I / 5], sK, so2); }
      });
      p1 = p2;
      if constexpr (PP == 0) STAMP(29);
      if constexpr (PP == 2) STAMP(30);
      if constexpr (PP == 4) STAMP(31);
    });
  }
  asm volatile("s_nop 7" ::: "memory");
  STAMP(2);
  const unsigned l16 = lb + lane * 16u;
  unsigned vv[4];
#pragma unroll
  for (unsigned c = 0; c < 4u; ++c) { vv[c] = v_voff + c * 3072u; PIN_V(vv[c]); }
  constexpr auto voff_c = [](int c) { return (c >> 1) * 32768u + (c & 1) * 4096u; };
  auto vunit = [&](int c) {
    set_m0_now(vbase + voff_c(c));
    forN<4>([&](auto i_c) { constexpr int I = decltype(i_c)::value; dma_kv<1024 * I>(vv[I], sV, c * 16384u); });
  };
#pragma unroll
  for (int u = 0; u < 2; ++u)
#pragma unroll
    for (int m = 0; m < 5; ++m) {
      PIN_V(qa[u][0][m]); PIN_V(qa[u][1][m]);
      lds_wr16(l16 + L_QX + w * 10240u + (u * 5 + m) * 1024u, pack8(qa[u][0][m], qa[u][1][m]));
    }
  vunit(0); vunit(1);
  barrier_lds();
  frag Q[8][5];
  forN<40>([&](auto i_c) { constexpr int I = decltype(i_c)::value; lds_rd16_acc<I * 1024>(Q[I / 5][I % 5], l16 + L_QX); });
#define QOPS(s) "+a"(Q[s][0]), "+a"(Q[s][1]), "+a"(Q[s][2]), "+a"(Q[s][3]), "+a"(Q[s][4])
  asm volatile("s_waitcnt lgkmcnt(0)\n\ts_barrier" : QOPS(0), QOPS(1), QOPS(2), QOPS(3) :: "memory");
  asm volatile("" : QOPS(4), QOPS(5), QOPS(6), QOPS(7) :: "memory");
#undef QOPS

  unsigned kr[4], vr[4];
  const unsigned fv = (2u * g + (q4 >> 1)) & 3u;
#pragma unroll
  for (unsigned c = 0; c < 4u; ++c) {
    kr[c] = kbase + r16 * 512u + (((4u * c + g) ^ r16) << 4);
    vr[c] = vbase + (4u * g + q4) * 128u + ((((c ^ fv) << 1) | (p4 >> 1)) << 4) + (p4 & 1u) * 8u;
    PIN_V(kr[c]); PIN_V(vr[c]);
  }
  unsigned pa = l16 + L_P, pw = pa + w * 5120u, ma = lb + L_MX + r16 * 4u;
  PIN_V(pa); PIN_V(pw); PIN_V(ma);

  float mrow[5], nm[5], lrow[5];
  frag P[5];

  frag ka[4], kb[4], pA[5], pB[5];
  u32x2 va[8], vb[8];
  auto rdK1 = [&](frag (&f)[4], int u, int half, int s) { f[s] = lds_rd16(kr[s] + u * 8192u + half * 256u); };
  auto rdV1 = [&](u32x2 (&f)[8], int c, int i) { f[i] = lds_tr1(vr[i >> 1] + voff_c(c) + (i & 1) * 2048u); };
  auto rdP1 = [&](frag (&f)[5], int c, int m) { f[m] = lds_rd16(pa + (c * 5 + m) * 1024u); };
  auto grpQK = [&](f32x4 (&S)[5], frag (&f)[4], auto half_c, auto fill) {
    constexpr int half = decltype(half_c)::value;
    forN<20>([&](auto i_c) {
      constexpr int I = decltype(i_c)::value, s = I / 5, m = I % 5;
      if (half == 0 && s == 0) mm_va0(S[m], f[s], Q[s][m]); else mm_va(S[m], f[s], Q[4 * half + s][m]);
      fill(i_c); SBAR();
    });
  };
  auto pv = [&](auto wait_fn, auto fill) {
    auto grp1 = [&](u32x2 (&vf)[8], frag (&pf)[5], u32x2 (&vn)[8], frag (&pn)[5], auto c_c) {
      constexpr int C = decltype(c_c)::value;
      wait_fn(c_c);
      SBAR();
      forN<20>([&](auto i_c) {
        constexpr int I = decltype(i_c)::value, m = I / 4, d = I % 4;
        mm_av(O[d][m], cat2(vf[2 * d], vf[2 * d + 1]), pf[m]);
        if constexpr (C < 3 && I < 8) rdV1(vn, C + 1, I);
        if constexpr (C < 3 && I >= 8 && I < 13) rdP1(pn, C + 1, I - 8);
        fill(c_c, i_c); SBAR();
      });
    };
    grp1(va, pA, vb, pB, IC<0>{}); grp1(vb, pB, va, pA, IC<1>{}); grp1(va, pA, vb, pB, IC<2>{}); grp1(vb, pB, va, pA, IC<3>{});
  };

  vunit(2); vunit(3);
  wait_vm<24>();
#pragma unroll
  for (int s = 0; s < 4; ++s) rdK1(ka, 0, 0, s);
  srd_t srdK = sK;
  unsigned kq0 = 65536u + w * 16384u;
  f32x4 S0[5], S1[5];
  float t0[5], t1[5], al[5] = {1.f, 1.f, 1.f, 1.f, 1.f};
  auto phaseY = [&](auto first_c, unsigned j, const unsigned (&kq)[4]) {
    constexpr bool FIRST = decltype(first_c)::value;
    float mx[5];
    auto kpiece = [&](auto q_c, auto i_c) {
      constexpr int Qd = decltype(q_c)::value, I = decltype(i_c)::value;
      dma_kv<1024 * I>(kv[Qd & 1][I], srdK, kq[Qd]);
    };
    SBAR();
    grpQK(S0, ka, IC<0>{}, [&](auto i_c) {
      constexpr int I = decltype(i_c)::value;
      if constexpr (I < 4) rdK1(kb, 0, 1, I);
      if constexpr (!FIRST && I >= 5 && I % 3 == 2) lds_wr16(pw + ((I - 5) / 3) * 1024u, P[(I - 5) / 3]);
    });
    if constexpr (FIRST) wait_vm<16>(); else wait_vm_lt<2, 0, 16>(j);
    grpQK(S0, kb, IC<1>{}, [&](auto i_c) {
      constexpr int I = decltype(i_c)::value;
      if constexpr (I < 4) rdK1(ka, 1, 0, I);
      if constexpr (I == 4) { wait_lgkm<4>(); set_m0(kbase); }
      if constexpr (I >= 5 && I % 4 == 1) kpiece(IC<0>{}, IC<(I - 5) / 4>{});
    });
    grpQK(S1, ka, IC<0>{}, [&](auto i_c) {
      constexpr int I = decltype(i_c)::value;
      if constexpr (I < 4) rdK1(kb, 1, 1, I);
      if constexpr (I == 0) set_m0(kbase + 4096u);
      if constexpr (I % 5 == 1) kpiece(IC<1>{}, IC<I / 5>{});
      if constexpr (I >= 8 && I < 13) mx[I - 8] = max3f(S0[I - 8][0], S0[I - 8][1], S0[I - 8][2]);
      if constexpr (I >= 13 && I < 18) mx[I - 13] = max2f(mx[I - 13], S0[I - 13][3]);
    });
    wait_lgkm<0>();
    if constexpr (!FIRST) wait_vm<20>();
    grpQK(S1, kb, IC<1>{}, [&](auto i_c) {
      constexpr int I = decltype(i_c)::value;
      if constexpr (!FIRST && I < 8) rdV1(va, 0, I);
      if constexpr (I == 0) set_m0(kbase + 8192u);
      if constexpr (I % 4 == 1 && I < 16) kpiece(IC<2>{}, IC<I / 4>{});
      if constexpr (I == 16) set_m0(kbase + 12288u);
      if constexpr (I == 17) kpiece(IC<3>{}, IC<0>{});
    });
    kpiece(IC<3>{}, IC<1>{});
    asm volatile("s_nop 6" ::: "memory");
    SBAR();
#pragma unroll
    for (int m = 0; m < 5; ++m) mx[m] = max3f(mx[m], S1[m][0], S1[m][1]);
#pragma unroll
    for (int m = 0; m < 5; ++m) mx[m] = max3f(mx[m], S1[m][2], S1[m][3]);
    SBAR();
    kpiece(IC<3>{}, IC<2>{});
    lds_max<0>(ma, mx[0]); lds_max<64>(ma, mx[1]); lds_max<128>(ma, mx[2]); lds_max<192>(ma, mx[3]); lds_max<256>(ma, mx[4]);
    kpiece(IC<3>{}, IC<3>{});
    barrier_lds();
  };
  auto e_fma = [&](auto e_c) { constexpr int E = decltype(e_c)::value; if constexpr (E % 8 < 4) S0[E / 8][E % 8] = fmaf(S0[E / 8][E % 8], SM_C, nm[E / 8]); else S1[E / 8][E % 8 - 4] = fmaf(S1[E / 8][E % 8 - 4], SM_C, nm[E / 8]); };
  auto e_exp = [&](auto e_c) { constexpr int E = decltype(e_c)::value; if constexpr (E % 8 < 4) S0[E / 8][E % 8] = __builtin_amdgcn_exp2f(S0[E / 8][E % 8]); else S1[E / 8][E % 8 - 4] = __builtin_amdgcn_exp2f(S1[E / 8][E % 8 - 4]); };
  auto e_fin = [&](auto o_c) {
    constexpr int Op = decltype(o_c)::value, m = Op % 5, st = Op / 5;
    if constexpr (st == 0) t0[m] = addf(S0[m][0], S0[m][1]);
    if constexpr (st == 1) t1[m] = addf(S0[m][2], S0[m][3]);
    if constexpr (st == 2) P[m][0] = cvtpk_here(S0[m][0], S0[m][1]);
    if constexpr (st == 3) P[m][1] = cvtpk_here(S0[m][2], S0[m][3]);
    if constexpr (st == 4) t0[m] = addf(t0[m], S1[m][0]);
    if constexpr (st == 5) t1[m] = addf(t1[m], S1[m][1]);
    if constexpr (st == 6) t0[m] = addf(t0[m], S1[m][2]);
    if constexpr (st == 7) t1[m] = addf(t1[m], S1[m][3]);
    if constexpr (st == 8) P[m][2] = cvtpk_here(S1[m][0], S1[m][1]);
    if constexpr (st == 9) { P[m][3] = cvtpk_here(S1[m][2], S1[m][3]); t0[m] = addf(t0[m], t1[m]); }
    if constexpr (st == 10) lrow[m] = addf(lrow[m], t0[m]);
  };
  {
    STAMP(3);
    const unsigned kq[4] = {kq0, kq0 + 4096u, kq0 + 8192u, kq0 + 12288u};
    phaseY(IC<1>{}, 0u, kq);
    STAMP(4);
#pragma unroll
    for (int m = 0; m < 5; ++m) { mrow[m] = lds_rdf(ma + m * 64u); nm[m] = -mrow[m] * SM_C; lrow[m] = 0.f; }
    SBAR();
    forN<40>(e_fma); SBAR();
    forN<40>(e_exp); SBAR();
    wait_vm<8>();
#pragma unroll
    for (int s = 0; s < 4; ++s) rdK1(ka, 0, 0, s);
    SBAR();
    forN<55>(e_fin);
    SBAR();
    barrier_raw();
  }
#pragma unroll 1
  for (unsigned j = 1; j < 8u; ++j) {
    STAMP(3 + 2 * j);
    kq0 += 65536u;
    unsigned kq[4];
    if (j == 7u) {
      srdK = sO;
      const unsigned woa = (lane >> 3) * 896u + (((lane & 7u) ^ (((lane >> 4) & 3u) << 1)) << 4) + w * 128u;
#pragma unroll
      for (int i = 0; i < 4; ++i) kv[0][i] = kv[1][i] = woa + i * 6144u;
    }
#pragma unroll
    for (unsigned q = 0; q < 4u; ++q) { kq[q] = j == 7u ? ((b + q) & 7u) * 28672u : kq0 + q * 4096u; PIN_S(kq[q]); }
    phaseY(IC<0>{}, j, kq);
    STAMP(4 + 2 * j);
    float cm[5];
#pragma unroll
    for (int m = 0; m < 5; ++m) cm[m] = lds_rdf(ma + m * 64u);
#pragma unroll
    for (int m = 0; m < 5; ++m) rdP1(pA, 0, m);
    float gr = max3f(cm[0] - mrow[0], cm[1] - mrow[1], cm[2] - mrow[2]);
    gr = max3f(gr, cm[3] - mrow[3], cm[4] - mrow[4]);
    const bool resc = __any(gr > RESCALE_THR / SM_C);
    if (__builtin_expect(resc, 0)) {
#pragma unroll
      for (int m = 0; m < 5; ++m) { al[m] = __builtin_amdgcn_exp2f((mrow[m] - cm[m]) * SM_C); mrow[m] = cm[m]; nm[m] = -cm[m] * SM_C; lrow[m] *= al[m]; }
    }
    const unsigned vso = j * 65536u;
    pv([&](auto c_c) { if constexpr (decltype(c_c)::value < 3) wait_vm<24>(); else wait_vm<20>(); },
       [&](auto c_c, auto i_c) {
         constexpr int C = decltype(c_c)::value, I = decltype(i_c)::value;
         if constexpr (C == 3 && I < 4) rdK1(ka, 0, 0, I);
         if constexpr (I == 3) set_m0(vbase + voff_c(C));
         if constexpr (I >= 4 && I % 4 == 0) dma_kv<1024 * (I / 4 - 1)>(vv[I / 4 - 1], sV, vso + C * 16384u);
         if constexpr (C == 0) { e_fma(IC<2 * I>{}); e_fma(IC<2 * I + 1>{}); }
         if constexpr (C == 1 || C == 2) e_exp(IC<20 * (C - 1) + I>{});
         if constexpr (C == 3) { e_fin(IC<2 * I>{}); e_fin(IC<2 * I + 1>{}); }
       });
    forN<15>([&](auto o_c) { e_fin(IC<40 + decltype(o_c)::value>{}); });
    if (__builtin_expect(resc, 0)) {
      asm volatile("s_nop 7" ::: "memory");
#pragma unroll
      for (int d = 0; d < 4; ++d)
#pragma unroll
        for (int m = 0; m < 5; ++m) { PIN_A(O[d][m]); O[d][m] *= al[m]; PIN_A(O[d][m]); }
    }
    SBAR();
    barrier_raw();
  }
  STAMP(19);
  const unsigned sa = lb + L_ST + r16 * 16u;
#pragma unroll
  for (int m = 0; m < 5; ++m) lds_wr16(pw + m * 1024u, P[m]);
#pragma unroll
  for (int m = 0; m < 5; ++m) lds_wrf(sa + m * 256u + w * 4u, xsum_g(lrow[m]));
  unsigned wob[3];
#pragma unroll
  for (unsigned i = 0; i < 3u; ++i) { const unsigned n = i * 64u + lane, row = (n * 171u) >> 10; wob[i] = row * 896u + (n - row * 6u) * 16u + 512u + w * 96u - i * 1024u; }
  f32x4 acc[7][5];
#pragma unroll
  for (int n = 0; n < 7; ++n)
#pragma unroll
    for (int m = 0; m < 5; ++m) { acc[n][m] = f32x4{0.f, 0.f, 0.f, 0.f}; PIN_V(acc[n][m]); }
  auto kso_of = [&](unsigned s) { return ((s + b) & 7u) * 28672u; };
  auto bslot = [&](int s) { return s < 2 ? vbase + 34816u + s * 3072u : (s < 4 ? lb + L_P + w * 6144u + (s - 2) * 3072u : kbase + (s - 4) * 4096u); };
  auto bunit = [&](int s) { set_m0_now(bslot(s)); dma_o<0>(wob[0], sO, kso_of(s)); dma_o<1024>(wob[1], sO, kso_of(s)); dma_o<2048>(wob[2], sO, kso_of(s)); };
  wait_vm<12>();
#pragma unroll
  for (int i = 0; i < 8; ++i) rdV1(va, 0, i);
  barrier_lds();
  STAMP(20);
  f32x4 st[5];
#pragma unroll
  for (int m = 0; m < 5; ++m) rdP1(pA, 0, m);
#pragma unroll
  for (int m = 0; m < 5; ++m) st[m] = lds_rdf4(sa + m * 256u);
  float inv[5];
  pv([&](auto c_c) { constexpr int C = decltype(c_c)::value; if constexpr (C < 3) wait_vm<8 - 4 * C>(); },
     [&](auto c_c, auto i_c) {
       constexpr int C = decltype(c_c)::value, I = decltype(i_c)::value;
       if constexpr (C == 3 && I < 5) inv[I] = (st[I][0] + st[I][1]) + (st[I][2] + st[I][3]);
       if constexpr (C == 3 && I >= 5 && I < 10) inv[I - 5] = __builtin_amdgcn_rcpf(inv[I - 5]);
     });
  bunit(0); bunit(1);
  STAMP(21);
#pragma unroll
  for (int m = 0; m < 5; ++m)
#pragma unroll
    for (int u = 0; u < 2; ++u) {
      constexpr auto foff = [](int f) { return f < 8 ? f * 1024u : 32768u + (f - 8) * 1024u; };
      PIN_A(O[2 * u][m]); PIN_A(O[2 * u + 1][m]);
      lds_wr16(l16 + L_V + w * 8192u + foff(u * 5 + m), pack8(O[2 * u][m] * inv[m], O[2 * u + 1][m] * inv[m]));
    }
  auto rdO = [&](frag (&f)[5], unsigned a0, unsigned a1, int m) { f[m] = lds_rd16((m < 3 ? a0 : a1) + m * 1024u); };
  auto oa0 = [&](unsigned s) { const unsigned k = (s + b) & 7u; return l16 + L_V + (k >> 1) * 8192u + (k & 1u) * 5120u; };
  auto oa1 = [&](unsigned s) { const unsigned k = (s + b) & 7u; return l16 + L_V + (k >> 1) * 8192u + (k & 1u) * 29696u; };
  u32x2 wf[8];
  frag oa[5], ob[5];
  unsigned ar[4];
#pragma unroll
  for (int c = 0; c < 4; ++c) ar[c] = vr[c] - vbase + kbase;
#pragma unroll
  for (int i = 0; i < 8; ++i) wf[i] = lds_tr1(ar[i >> 1] + (i & 1) * 2048u);
  barrier_lds();
  STAMP(22);
#pragma unroll
  for (int m = 0; m < 5; ++m) rdO(oa, oa0(0u), oa1(0u), m);
  bunit(2); bunit(3);
  {
    unsigned fv0 = kv[0][0], fv1 = kv[0][1], fv2 = kv[0][2];
    auto step = [&](frag (&oc)[5], frag (&on)[5], unsigned s, auto odd_c) {
      constexpr unsigned ODD = decltype(odd_c)::value;
      unsigned slot = kbase + (s & 3u) * 4096u, so = kso_of(s < 4u ? s + 4u : s), a0 = oa0(s + 1u), a1 = oa1(s + 1u);
      PIN_S(slot); PIN_S(so); PIN_V(a0); PIN_V(a1);
      if (ODD) {
        const unsigned d = (s & 2u) ? 0u - 8192u : 8192u;
#pragma unroll
        for (int c = 0; c < 4; ++c) { ar[c] += d; PIN_V(ar[c]); }
      }
      wait_vm_lt<3, 63, 6>(s);
      SBAR();
      forN<20>([&](auto i_c) {
        constexpr int I = decltype(i_c)::value, n = I / 5, m = I % 5;
        mm_vv(acc[n][m], cat2(wf[2 * n], wf[2 * n + 1]), oc[m]);
        if constexpr (I < 4) rdO(on, a0, a1, I);
        if constexpr (I == 6) rdO(on, a0, a1, 4);
        if constexpr (m == 4) wf[2 * n] = lds_tr1(ar[n] + (1u - ODD) * 4096u);
        if constexpr ((m == 0 && n > 0) || I == 19) wf[I == 19 ? 7 : 2 * n - 1] = lds_tr1(ar[I == 19 ? 3 : n - 1] + (1u - ODD) * 4096u + 2048u);
        if constexpr (I == 7) { wait_lgkm<7>(); set_m0(slot); }
        if constexpr (I == 8) dma_o<0>(fv0, sO, so);
        if constexpr (I == 11) dma_o<1024>(fv1, sO, so);
        if constexpr (I == 13) dma_o<2048>(fv2, sO, so);
        if constexpr (I == 17) dma_o_lt<3072, 4>(s, kv[0][3], sO, so);
        SBAR();
      });
    };
#pragma unroll 1
    for (unsigned s = 0; s < 8u; s += 2u) {
      if (s == 4u) { fv0 = wob[0]; fv1 = wob[1]; fv2 = wob[2]; }
      step(oa, ob, s, IC<0>{}); step(ob, oa, s + 1u, IC<1>{});
    }
  }
  STAMP(28);
  const srd_t sP = mk_srd(partp, HQ * PART_B);
  const unsigned po = h * PART_B + lane * 16u;
  {
    const unsigned brd = (4u * g + q4) * 96u + p4 * 8u;
    u32x2 wg[6];
#pragma unroll
    for (int i = 0; i < 6; ++i) wg[i] = lds_tr1(bslot(0) + brd + (i >> 1) * 32u + (i & 1) * 1536u);
    forN<8>([&](auto s_c) {
      constexpr int S = decltype(s_c)::value;
      frag (&oc)[5] = (S & 1) ? ob : oa;
      frag (&on)[5] = (S & 1) ? oa : ob;
      unsigned a0 = oa0(S + 1u), a1 = oa1(S + 1u), an = bslot(S < 7 ? S + 1 : 0) + brd;
      PIN_V(a0); PIN_V(a1); PIN_V(an);
      wait_vm<10>();
      SBAR();
      forN<15>([&](auto i_c) {
        constexpr int I = decltype(i_c)::value, n = I / 5, m = I % 5;
        mm_vv(acc[4 + n][m], cat2(wg[2 * n], wg[2 * n + 1]), oc[m]);
        if constexpr (S < 7) {
          if constexpr (I < 4) rdO(on, a0, a1, I);
          if constexpr (I == 6) rdO(on, a0, a1, 4);
          if constexpr (m == 4) wg[2 * n] = lds_tr1(an + n * 32u);
          if constexpr ((m == 0 && n > 0) || I == 14) wg[I == 14 ? 5 : 2 * n - 1] = lds_tr1(an + (I == 14 ? 2 : n - 1) * 32u + 1536u);
        }
        if constexpr (S < 5 && (I == 8 || I == 13)) {
          constexpr int U = 2 * S + (I == 13), T = 2 * U;
          auto& a = acc[T / 5][T % 5]; auto& c = acc[(T + 1) / 5][(T + 1) % 5];
          PIN_V(a); PIN_V(c);
          __builtin_amdgcn_raw_buffer_store_b128(pack8(a, c), sP, po + U * 1024u, w * 10240u, PART_AUX);
        }
        SBAR();
      });
    });
  }
  asm volatile("s_nop 7" ::: "memory");
  STAMP(24);
  unsigned long long ackv;
  asm volatile("global_load_dwordx2 %0, %1, %2 sc1" : "=v"(ackv) : "v"(r16 * 128u + h * 8u), "s"(ackp) : "memory");
#pragma unroll
  for (int i = 0; i < 7; ++i) {
    auto& a = acc[4 + (2 * i) / 5][(2 * i) % 5]; auto& c = acc[4 + (2 * i + 1) / 5][(2 * i + 1) % 5];
    PIN_V(a); PIN_V(c);
    __builtin_amdgcn_raw_buffer_store_b128(pack8(a, c), sP, po + i * 1024u, 40960u + w * 7168u, PART_AUX);
  }
  PIN_V(acc[6][4]);
  __builtin_amdgcn_raw_buffer_store_b64(u32x2{cvtpk(acc[6][4][0], acc[6][4][1]), cvtpk(acc[6][4][2], acc[6][4][3])}, sP, po + (w & 1u) * 8u, (68u + (w >> 1)) * 1024u, PART_AUX);
  unsigned lo[5], oo[5];
#pragma unroll
  for (int t = 0; t < 5; ++t) {
    const unsigned rho = h * 4u + w, c = 16u * (t < 3 ? t : t - 3) + r16, hf = g & 1u;
    const bool ok = c < (t < 3 ? 40u : 30u);
    const unsigned gm = t < 3 ? rho * 40u + c : 2560u + rho * 30u + c, U = gm >> 6, l2 = gm & 63u;
    unsigned col, tl;
    if (t < 3) { const unsigned wv = (U * 26u) >> 8; tl = 2u * (U - 10u * wv) + hf; col = 64u * wv; }
    else { const unsigned u2 = U - 40u, wv = u2 < 28u ? (u2 * 37u) >> 8 : 2u * (u2 - 28u) + hf; tl = u2 < 28u ? 2u * (u2 - 7u * wv) + hf : 14u; col = 256u + 48u * wv; }
    const unsigned n = (tl * 52u) >> 8, m = tl - 5u * n;
    lo[t] = ok ? g * PART_B + gm * 16u : 0x40000000u;
    oo[t] = ok && g < 2u ? (16u * m + (l2 & 15u)) * (2 * DM * 2) + (col + 16u * n + 4u * (l2 >> 4)) * 2u : 0x40000000u;
  }
  asm volatile("s_waitcnt vmcnt(0)" : "+v"(ackv) :: "memory");
  barrier_raw();
  STAMP(25);
  if (w == 0u) {
    const unsigned long long mine = ((unsigned long long)(unsigned)__builtin_amdgcn_readlane((unsigned)(tag >> 32), h) << 32) | (unsigned)__builtin_amdgcn_readlane((unsigned)tag, h);
    for (unsigned spin = 0; !__all(ackv == mine) && spin < (1u << 18); ++spin) {
      __builtin_amdgcn_s_sleep(1);
      ackv = __hip_atomic_load((gu64*)(ackp + r16 * 128u + h * 8u), RLX_AGENT);
    }
    if (lane == 0u) __hip_atomic_store((gu64*)(flagp + h * 8u), mine, RLX_AGENT);
    for (unsigned spin = 0; spin < (1u << 18); ++spin) {
      if (__all(__hip_atomic_load((gu64*)(flagp + r16 * 8u), RLX_AGENT) == tag)) break;
      __builtin_amdgcn_s_sleep(1);
    }
  }
  barrier_raw();
  STAMP(26);
  {
    frag pf[5][4];
#pragma unroll
    for (int t = 0; t < 5; ++t)
#pragma unroll
      for (int k = 0; k < 4; ++k) pf[t][k] = __builtin_amdgcn_raw_buffer_load_b128(sP, lo[t], k * 4u * PART_B, PART_AUX);
    frag sel;
#pragma unroll
    for (int d = 0; d < 4; ++d) sel[d] = (r16 >> 1) == (unsigned)d ? ((r16 & 1u) ? 0x3F800000u : 0x00003F80u) : 0u;
    const srd_t sOut = mk_srd(outp, SQ * 2 * DM * 2 - br * (DM * 2));
#pragma unroll
    for (int t = 0; t < 5; ++t) {
      f32x4 r = {0.f, 0.f, 0.f, 0.f};
#pragma unroll
      for (int k = 0; k < 4; ++k) r = __builtin_amdgcn_mfma_f32_16x16x32_bf16(__builtin_bit_cast(bf16x8, sel), __builtin_bit_cast(bf16x8, pf[t][k]), r, 0, 0, 0);
      __builtin_amdgcn_raw_buffer_store_b64(u32x2{cvtpk(r[0], r[1]), cvtpk(r[2], r[3])}, sOut, oo[t], 0, 0);
    }
  }
  STAMP(27);
  STAMP_FLUSH();
}

extern "C" void kernel_launch(void* const* d_in, const int* in_sizes, int n_in, void* d_out, int out_size, void* d_ws, size_t ws_size,
                              hipStream_t stream) {
  gqa_block_fused<<<256, 256, 0, stream>>>((const char*)d_in[0], (const char*)d_in[1], (const char*)d_in[2], (const char*)d_in[3], (const char*)d_in[4],
                                           (const char*)d_in[5], (const char*)d_in[6], (const char*)d_in[7], (const char*)d_in[8], (const char*)d_in[9],
                                           (char*)d_ws, (char*)d_out);
}
```
